```python
import torch

def _softmax(x, axis=-1):
    return torch.softmax(x, dim=axis)

def _gelu(x):
    return torch.nn.functional.gelu(x, approximate='tanh')

def _silu(x):
    return torch.nn.functional.silu(x)

def _sigmoid(x):
    return torch.sigmoid(x)

def _layernorm(x, axis=-1, eps=1e-05):
    m = x.mean(dim=axis, keepdim=True)
    v = x.var(dim=axis, keepdim=True, unbiased=False)
    return (x - m) / torch.sqrt(v + eps)

def _scatter_add(tgt, idx, src, axis=0):
    return tgt.clone().index_add_(axis, idx, src)

def _segment_sum(src, seg, n):
    out = torch.zeros((n,) + src.shape[1:], dtype=src.dtype)
    return out.index_add_(0, seg, src)

def _pad(x, pw):
    flat = tuple((v for pair in reversed(pw) for v in pair))
    return torch.nn.functional.pad(x, flat)
kernel_name = 'standalone__conformer_conv_module__speech'

def setup_inputs():

    _w = lambda a: (lambda t: t.to(torch.bfloat16) if t.is_floating_point() else t)(torch.as_tensor(a))
    B, T, D, K = (2, 4000, 512, 15)
    return {'x': _w((lambda *s: torch.randn(*s, dtype=torch.bfloat16) * 0.2)(B, T, D)), 'w_pw1': _w((lambda *s: torch.randn(*s, dtype=torch.bfloat16) * 0.2)(D, 2 * D)), 'dw': _w((lambda *s: torch.randn(*s, dtype=torch.bfloat16) * 0.2)(K, D)), 'w_pw2': _w((lambda *s: torch.randn(*s, dtype=torch.bfloat16) * 0.05)(D, D))}

def reference(x, w_pw1, dw, w_pw2):
    B, T, D = x.shape
    K = dw.shape[0]
    h = x @ w_pw1
    a, b = (h[..., :D].clone(), h[..., D:].clone())
    h = a * _sigmoid(b)
    pad = K // 2
    hp = _pad(h, ((0, 0), (pad, pad), (0, 0)))
    out = torch.zeros_like(h)
    for i in range(K):
        out = out + hp[:, i:i + T, :] * dw[i][None, None, :]
    return _silu(out) @ w_pw2
```

```cpp
#include <hip/hip_runtime.h>
#include <hip/hip_bf16.h>
#include <stdint.h>

typedef __attribute__((ext_vector_type(8))) short bf16x8;
typedef __attribute__((ext_vector_type(4))) float f32x4;

static __device__ __forceinline__ float bf2f(unsigned short u) { return __uint_as_float(((unsigned)u) << 16); }
static __device__ __forceinline__ unsigned short f2bf(float f) {
  unsigned u = __float_as_uint(f);
  u += 0x7fff + ((u >> 16) & 1);
  return (unsigned short)(u >> 16);
}

__global__ void k_gemm1(const unsigned short* __restrict__ x, const unsigned short* __restrict__ w1,
                        unsigned short* __restrict__ h) {
  int wave = (blockIdx.x * blockDim.x + threadIdx.x) >> 6;
  int l = threadIdx.x & 63;
  int mt = wave / 32, nt = wave % 32;
  f32x4 a = {0, 0, 0, 0}, b = {0, 0, 0, 0};
  for (int k0 = 0; k0 < 512; k0 += 32) {
    bf16x8 af = *(const bf16x8*)(x + (size_t)(mt * 16 + (l & 15)) * 512 + k0 + 8 * (l >> 4));
    bf16x8 ba, bb;
    for (int j = 0; j < 8; j++) {
      ba[j] = w1[(size_t)(k0 + 8 * (l >> 4) + j) * 1024 + nt * 16 + (l & 15)];
      bb[j] = w1[(size_t)(k0 + 8 * (l >> 4) + j) * 1024 + 512 + nt * 16 + (l & 15)];
    }
    a = __builtin_amdgcn_mfma_f32_16x16x32_bf16(af, ba, a, 0, 0, 0);
    b = __builtin_amdgcn_mfma_f32_16x16x32_bf16(af, bb, b, 0, 0, 0);
  }
  for (int r = 0; r < 4; r++) {
    float v = a[r] / (1.f + __expf(-b[r]));
    h[(size_t)(mt * 16 + (l >> 4) * 4 + r) * 512 + nt * 16 + (l & 15)] = f2bf(v);
  }
}

__global__ void k_conv(const unsigned short* __restrict__ h, const unsigned short* __restrict__ dw,
                       unsigned short* __restrict__ s) {
  int idx = blockIdx.x * blockDim.x + threadIdx.x;
  int c = idx & 511, row = idx >> 9;
  int bt = row / 4000, t = row % 4000;
  float acc = 0;
  for (int i = 0; i < 15; i++) {
    int tt = t + i - 7;
    if (tt >= 0 && tt < 4000) acc += bf2f(h[(size_t)(bt * 4000 + tt) * 512 + c]) * bf2f(dw[i * 512 + c]);
  }
  s[idx] = f2bf(acc / (1.f + __expf(-acc)));
}

__global__ void k_gemm2(const unsigned short* __restrict__ s, const unsigned short* __restrict__ w2,
                        unsigned short* __restrict__ out) {
  int wave = (blockIdx.x * blockDim.x + threadIdx.x) >> 6;
  int l = threadIdx.x & 63;
  int mt = wave / 32, nt = wave % 32;
  f32x4 a = {0, 0, 0, 0};
  for (int k0 = 0; k0 < 512; k0 += 32) {
    bf16x8 af = *(const bf16x8*)(s + (size_t)(mt * 16 + (l & 15)) * 512 + k0 + 8 * (l >> 4));
    bf16x8 ba;
    for (int j = 0; j < 8; j++) ba[j] = w2[(size_t)(k0 + 8 * (l >> 4) + j) * 512 + nt * 16 + (l & 15)];
    a = __builtin_amdgcn_mfma_f32_16x16x32_bf16(af, ba, a, 0, 0, 0);
  }
  for (int r = 0; r < 4; r++) out[(size_t)(mt * 16 + (l >> 4) * 4 + r) * 512 + nt * 16 + (l & 15)] = f2bf(a[r]);
}

extern "C" void kernel_launch(void* const* d_in, const int* in_sizes, int n_in, void* d_out, int out_size,
                              void* d_ws, size_t ws_size, hipStream_t stream) {
  const unsigned short* x = (const unsigned short*)d_in[0];
  const unsigned short* w1 = (const unsigned short*)d_in[1];
  const unsigned short* dw = (const unsigned short*)d_in[2];
  const unsigned short* w2 = (const unsigned short*)d_in[3];
  unsigned short* h = (unsigned short*)d_ws;
  unsigned short* s = h + 8000 * 512;
  k_gemm1<<<500 * 32 / 4, 256, 0, stream>>>(x, w1, h);
  k_conv<<<8000 * 512 / 256, 256, 0, stream>>>(h, dw, s);
  k_gemm2<<<500 * 32 / 4, 256, 0, stream>>>(s, w2, (unsigned short*)d_out);
}
```

```cpp
#include <hip/hip_runtime.h>
#include <stdint.h>

typedef unsigned short u16;
typedef __attribute__((ext_vector_type(8))) short bf16x8;
typedef __attribute__((ext_vector_type(4))) short s16x4;
typedef __attribute__((ext_vector_type(4))) float f32x4;
typedef __attribute__((ext_vector_type(2))) float f32x2;
typedef __attribute__((ext_vector_type(2))) __bf16 bf16x2;
typedef __attribute__((ext_vector_type(4))) unsigned u32x4;
typedef __attribute__((ext_vector_type(2))) unsigned u32x2;
#define LDSP(T) __attribute__((address_space(3))) T*
#define GLBP(T) __attribute__((address_space(1))) T*

#define WAIT_V(n) asm volatile("s_waitcnt vmcnt(%0)" ::"n"(n) : "memory")
#define WAIT_L(n) asm volatile("s_waitcnt lgkmcnt(%0)" ::"n"(n) : "memory")
#define BAR() asm volatile("s_barrier" ::: "memory")
#define SCHED() __builtin_amdgcn_sched_barrier(0)

__device__ __forceinline__ unsigned pack_bf16(float a, float b) {
  f32x2 v = {a, b};
  return __builtin_bit_cast(unsigned, __builtin_convertvector(v, bf16x2));
}
__device__ __forceinline__ float sigmoid_mul(float a, float b) {
  return a * __builtin_amdgcn_rcpf(1.f + __builtin_amdgcn_exp2f(b * -1.44269504088896f));
}
__device__ __forceinline__ void glds16(const void* sbase, unsigned voff, unsigned lds) {
  asm volatile("s_mov_b32 m0, %0\n\ts_nop 0\n\tglobal_load_lds_dwordx4 %1, %2" ::"s"(lds), "v"(voff), "s"(sbase));
}
__device__ __forceinline__ unsigned lds_addr(const char* p) { return (unsigned)(size_t)(LDSP(const char))p; }
__device__ __forceinline__ bf16x8 lds_b128(const char* p) { return *(const bf16x8*)p; }
__device__ __forceinline__ bf16x8 lds_tr2(const char* p) {
  s16x4 lo = __builtin_amdgcn_ds_read_tr16_b64_v4i16((LDSP(s16x4))p);
  s16x4 hi = __builtin_amdgcn_ds_read_tr16_b64_v4i16((LDSP(s16x4))(p + 1024));
  return __builtin_shufflevector(lo, hi, 0, 1, 2, 3, 4, 5, 6, 7);
}

constexpr int K1_XB = 272 * 128;
constexpr int K1_WB = 64 * 256;
constexpr int K1_ST = K1_XB + K1_WB;
constexpr int K1_TAB = 3 * K1_ST;
constexpr int K1_LDS = K1_TAB + 8192;
constexpr int HT_STRIDE = 560;

template <int NT, int NG>
__device__ __forceinline__ void k1_body(char* sm, const char* x, const char* w1, const unsigned (&xoff)[5],
                                        const unsigned (&woff)[2], int w, int l, int t0) {
  const int g = l >> 4, m = l & 15;
  const int mt0 = (NT == 9) ? 0 : 9;
  auto issue = [&](int kt, int st) {
    const char* xs = x + kt * 128;
    const char* ws = w1 + kt * (64 * 2048);
    unsigned d = lds_addr(sm) + st;
#pragma unroll
    for (int i = 0; i < 2; i++) glds16(ws, woff[i], d + K1_XB + (2 * w + i) * 1024);
#pragma unroll
    for (int i = 0; i < 4; i++) glds16(xs, xoff[i], d + (w + 8 * i) * 1024);
    if (NG == 7) glds16(xs, xoff[4], d + (32 + w - 6) * 1024);
  };
  const int a_off0 = (mt0 * 16 + m) * 128 + ((g ^ (l & 7)) << 4);
  const int a_off1 = a_off0 ^ 64;
  const int q = m >> 2, p = m & 3, wn = w & 3;
  const int f = q | ((g & 1) << 2);
  const int b_offa = K1_XB + (8 * g + q) * 256 + ((wn ^ f) << 5) + p * 8;
  const int b_offb = b_offa ^ 128;

  f32x4 acca[NT], accb[NT];
#pragma unroll
  for (int i = 0; i < NT; i++) acca[i] = f32x4{0, 0, 0, 0}, accb[i] = f32x4{0, 0, 0, 0};
  bf16x8 A0[NT], A1[NT], Ba0, Bb0, Ba1, Bb1;

  WAIT_V(2 * NG);
  BAR();
  Ba0 = lds_tr2(sm + b_offa);
  Bb0 = lds_tr2(sm + b_offb);
#pragma unroll
  for (int i = 0; i < NT; i++) A0[i] = lds_b128(sm + a_off0 + i * 2048);
  int st = 0;
#pragma unroll 1
  for (int it = 0; it < 8; it++) {
    const char* sc = sm + st;
    Ba1 = lds_tr2(sc + b_offa + 8192);
    Bb1 = lds_tr2(sc + b_offb + 8192);
#pragma unroll
    for (int i = 0; i < NT; i++) A1[i] = lds_b128(sc + a_off1 + i * 2048);
#pragma unroll
    for (int i = 0; i < NT; i++) {
      acca[i] = __builtin_amdgcn_mfma_f32_16x16x32_bf16(A0[i], Ba0, acca[i], 0, 0, 0);
      accb[i] = __builtin_amdgcn_mfma_f32_16x16x32_bf16(A0[i], Bb0, accb[i], 0, 0, 0);
    }
#pragma unroll
    for (int i = 0; i < NT + 4; i++) {
      __builtin_amdgcn_sched_group_barrier(0x008, 1, 0);
      __builtin_amdgcn_sched_group_barrier(0x100, 1, 0);
    }
    __builtin_amdgcn_sched_group_barrier(0x008, NT - 4, 0);
    SCHED();
    int stn = (st == 2 * K1_ST) ? 0 : st + K1_ST;
    if (it < 7) {
      WAIT_L(0);
      if (it < 6) WAIT_V(NG);
      else WAIT_V(0);
      BAR();
      if (it < 5) issue(it + 3, st);
    }
    SCHED();
    const char* sn = sm + stn;
    Ba0 = lds_tr2(sn + b_offa);
    Bb0 = lds_tr2(sn + b_offb);
#pragma unroll
    for (int i = 0; i < NT; i++) A0[i] = lds_b128(sn + a_off0 + i * 2048);
#pragma unroll
    for (int i = 0; i < NT; i++) {
      acca[i] = __builtin_amdgcn_mfma_f32_16x16x32_bf16(A1[i], Ba1, acca[i], 0, 0, 0);
      accb[i] = __builtin_amdgcn_mfma_f32_16x16x32_bf16(A1[i], Bb1, accb[i], 0, 0, 0);
    }
#pragma unroll
    for (int i = 0; i < NT + 4; i++) {
      __builtin_amdgcn_sched_group_barrier(0x008, 1, 1);
      __builtin_amdgcn_sched_group_barrier(0x100, 1, 1);
    }
    __builtin_amdgcn_sched_group_barrier(0x008, NT - 4, 1);
    SCHED();
    st = stn;
  }
  char* ht = sm + (wn * 16 + m) * HT_STRIDE + (mt0 * 16 + 4 * g) * 2;
  const int tb = t0 - 7 + mt0 * 16 + 4 * g;
#pragma unroll
  for (int i = 0; i < NT; i++) {
    float h[4];
#pragma unroll
    for (int r = 0; r < 4; r++) {
      float v = sigmoid_mul(acca[i][r], accb[i][r]);
      h[r] = ((unsigned)(tb + 16 * i + r) < 4000u) ? v : 0.f;
    }
    u32x2 pk = {pack_bf16(h[0], h[1]), pack_bf16(h[2], h[3])};
    *(u32x2*)(ht + i * 32) = pk;
  }
}

__global__ __launch_bounds__(512, 2) void k1(const u16* __restrict__ x, const u16* __restrict__ w1,
                                            const u16* __restrict__ dw, char* __restrict__ s) {
  extern __shared__ __attribute__((aligned(16))) char sm[];
  const int tid = threadIdx.x, l = tid & 63, w = __builtin_amdgcn_readfirstlane(tid >> 6);
  const int b = blockIdx.x, j = b >> 3, rb = (b & 7) * 4 + (j >> 3), cs = j & 7;
  const int batch = rb >> 4, t0 = (rb & 15) * 250;

  unsigned tap[8];
  const int idx0 = (w & 3) * 8 - 8 + (w >> 2);
  {
    const unsigned voff = (cs * 64 + l) * 2;
#pragma unroll
    for (int e = 0; e < 8; e++) {
      int i = idx0 + e;
      const u16* p = dw + ((unsigned)i < 15u ? i : 0) * 512;
      asm volatile("global_load_ushort %0, %1, %2" : "=v"(tap[e]) : "v"(voff), "s"(p));
    }
  }
  unsigned xoff[5], woff[2];
#pragma unroll
  for (int i = 0; i < 5; i++) {
    int rowm = 8 * (i < 4 ? (w + 8 * i) : (32 + (w & 1))) + (l >> 3);
    int t = min(max(t0 - 7 + rowm, 0), 3999);
    xoff[i] = (unsigned)((batch * 4000 + t) * 1024 + (((l & 7) ^ (l >> 3)) << 4));
  }
#pragma unroll
  for (int i = 0; i < 2; i++) {
    int row = 8 * w + 4 * i + (l >> 4);
    int f = (l >> 4) | ((w & 1) << 2);
    int slot = ((l & 15) >> 1) ^ f;
    int col = (slot & 3) * 16 + (l & 1) * 8 + cs * 64 + (slot >> 2) * 512;
    woff[i] = (unsigned)((row * 1024 + col) * 2);
  }
  const char* xb = (const char*)x;
  const char* wb = (const char*)w1;
#pragma unroll
  for (int kt = 0; kt < 3; kt++) {
    const char* xs = xb + kt * 128;
    const char* ws = wb + kt * (64 * 2048);
    unsigned d = lds_addr(sm) + kt * K1_ST;
#pragma unroll
    for (int i = 0; i < 2; i++) glds16(ws, woff[i], d + K1_XB + (2 * w + i) * 1024);
#pragma unroll
    for (int i = 0; i < 4; i++) glds16(xs, xoff[i], d + (w + 8 * i) * 1024);
    if (w >= 6) glds16(xs, xoff[4], d + (32 + w - 6) * 1024);
  }
  {
    asm volatile("s_waitcnt vmcnt(18)"
                 : "+v"(tap[0]), "+v"(tap[1]), "+v"(tap[2]), "+v"(tap[3]), "+v"(tap[4]), "+v"(tap[5]), "+v"(tap[6]),
                   "+v"(tap[7]));
#pragma unroll
    for (int e = 0; e < 8; e++) tap[e] = ((unsigned)(idx0 + e) < 15u) ? tap[e] : 0u;
    u32x4 tv = {tap[0] | (tap[1] << 16), tap[2] | (tap[3] << 16), tap[4] | (tap[5] << 16), tap[6] | (tap[7] << 16)};
    *(u32x4*)(sm + K1_TAB + l * 128 + w * 16) = tv;
  }
  if (w < 4) k1_body<9, 6>(sm, xb, wb, xoff, woff, w, l, t0);
  else if (w < 6) k1_body<8, 6>(sm, xb, wb, xoff, woff, w, l, t0);
  else k1_body<8, 7>(sm, xb, wb, xoff, woff, w, l, t0);
  WAIT_L(0);
  BAR();
  const int g = l >> 4, m = l & 15;
  int start = 8 * g - m;
  if (start < -8 || start > 14) start = -8;
  const char* tp = sm + K1_TAB + (8 * w) * 128 + (start & 1) * 64 + (((start + 8) >> 1) << 2);
  const char* hp = sm + (8 * w) * HT_STRIDE + 32 * m + 16 * g;
  f32x4 o[8];
#pragma unroll
  for (int c = 0; c < 8; c++) {
    const unsigned* t32 = (const unsigned*)(tp + c * 128);
    u32x4 av = {t32[0], t32[1], t32[2], t32[3]};
    bf16x8 hv = lds_b128(hp + c * HT_STRIDE);
    o[c] = __builtin_amdgcn_mfma_f32_16x16x32_bf16(__builtin_bit_cast(bf16x8, av), hv, f32x4{0, 0, 0, 0}, 0, 0, 0);
  }
  char* sp = s + (size_t)(((rb * 2 + (m >> 3)) * 64 + cs * 8 + w) * 2048 + g * 128 + (m & 7) * 16);
#pragma unroll
  for (int r = 0; r < 4; r++) {
    u32x4 v;
#pragma unroll
    for (int c = 0; c < 4; c++)
      v[c] = pack_bf16(sigmoid_mul(o[2 * c][r], o[2 * c][r]), sigmoid_mul(o[2 * c + 1][r], o[2 * c + 1][r]));
    *(u32x4*)(sp + r * 512) = v;
  }
}

constexpr int K2_NS = 5;
constexpr int K2_ST = 32768;
constexpr int K2_LDS = K2_NS * K2_ST;

__global__ __launch_bounds__(512, 2) void k2(const char* __restrict__ s, const u16* __restrict__ w2,
                                            u16* __restrict__ out) {
  extern __shared__ __attribute__((aligned(16))) char sm[];
  const int tid = threadIdx.x, l = tid & 63, w = __builtin_amdgcn_readfirstlane(tid >> 6);
  const int b = blockIdx.x, j = b >> 3, rb = (b & 7) * 4 + (j >> 3), half = (j >> 2) & 1, ns = j & 3;
  const char* sa = s + (size_t)(rb * 2 + half) * 131072 + w * 2048;
  const char* wb = (const char*)w2;
  unsigned woff[2];
#pragma unroll
  for (int i = 0; i < 2; i++) {
    int row = 8 * w + 4 * i + (l >> 4);
    int f = (l >> 4) | ((w & 1) << 2);
    int slot = ((l & 15) >> 1) ^ f;
    woff[i] = (unsigned)((row * 512 + ns * 128 + slot * 16 + (l & 1) * 8) * 2);
  }
  const unsigned aoff = l * 16;
  auto issue = [&](int kt) {
    unsigned d = lds_addr(sm) + (kt % K2_NS) * K2_ST;
#pragma unroll
    for (int i = 0; i < 2; i++) glds16(wb + kt * 65536, woff[i], d + 16384 + (2 * w + i) * 1024);
#pragma unroll
    for (int i = 0; i < 2; i++) glds16(sa + kt * 16384 + i * 1024, aoff, d + (2 * w + i) * 1024);
  };
#pragma unroll
  for (int kt = 0; kt < K2_NS; kt++) issue(kt);

  const int g = l >> 4, u = l & 15, q = u >> 2, p = u & 3, wm = w >> 2, wn = w & 3;
  const int f = q | ((g & 1) << 2);
  const int a_off = g * 2048 + wm * 1024 + u * 16;
  const int b_off0 = 16384 + (8 * g + q) * 256 + (((2 * wn) ^ f) << 5) + p * 8;
  const int b_off1 = b_off0 ^ 32;
  f32x4 acc[4][2];
#pragma unroll
  for (int i = 0; i < 4; i++) acc[i][0] = acc[i][1] = f32x4{0, 0, 0, 0};
  bf16x8 S0[4], S1[4], W00, W01, W10, W11;
  WAIT_V(4 * (K2_NS - 1));
  BAR();
  W00 = lds_tr2(sm + b_off0);
  W01 = lds_tr2(sm + b_off1);
#pragma unroll
  for (int i = 0; i < 4; i++) S0[i] = lds_b128(sm + a_off + i * 256);
#pragma unroll
  for (int it = 0; it < 8; it++) {
    const char* sc = sm + (it % K2_NS) * K2_ST;
    W10 = lds_tr2(sc + b_off0 + 8192);
    W11 = lds_tr2(sc + b_off1 + 8192);
#pragma unroll
    for (int i = 0; i < 4; i++) S1[i] = lds_b128(sc + a_off + 8192 + i * 256);
#pragma unroll
    for (int i = 0; i < 4; i++) {
      acc[i][0] = __builtin_amdgcn_mfma_f32_16x16x32_bf16(W00, S0[i], acc[i][0], 0, 0, 0);
      acc[i][1] = __builtin_amdgcn_mfma_f32_16x16x32_bf16(W01, S0[i], acc[i][1], 0, 0, 0);
    }
#pragma unroll
    for (int i = 0; i < 8; i++) {
      __builtin_amdgcn_sched_group_barrier(0x008, 1, 0);
      __builtin_amdgcn_sched_group_barrier(0x100, 1, 0);
    }
    SCHED();
    if (it < 7) {
      WAIT_L(0);
      constexpr int lastis = 7;
      int hi = it + K2_NS - 1 < lastis ? it + K2_NS - 1 : lastis;
      switch (hi - (it + 1)) {
        case 0: WAIT_V(0); break;
        case 1: WAIT_V(4); break;
        case 2: WAIT_V(8); break;
        case 3: WAIT_V(12); break;
        default: WAIT_V(16); break;
      }
      BAR();
      if (it + K2_NS < 8) issue(it + K2_NS);
      SCHED();
      const char* sn = sm + ((it + 1) % K2_NS) * K2_ST;
      W00 = lds_tr2(sn + b_off0);
      W01 = lds_tr2(sn + b_off1);
#pragma unroll
      for (int i = 0; i < 4; i++) S0[i] = lds_b128(sn + a_off + i * 256);
    }
#pragma unroll
    for (int i = 0; i < 4; i++) {
      acc[i][0] = __builtin_amdgcn_mfma_f32_16x16x32_bf16(W10, S1[i], acc[i][0], 0, 0, 0);
      acc[i][1] = __builtin_amdgcn_mfma_f32_16x16x32_bf16(W11, S1[i], acc[i][1], 0, 0, 0);
    }
#pragma unroll
    for (int i = 0; i < 8; i++) {
      __builtin_amdgcn_sched_group_barrier(0x008, 1, 1);
      __builtin_amdgcn_sched_group_barrier(0x100, 1, 1);
    }
    SCHED();
  }
  const int col = ns * 128 + wn * 32 + (g & 1) * 16 + (g >> 1) * 8;
#pragma unroll
  for (int i = 0; i < 4; i++) {
    const int jt = 4 * wm + i;
    const int tl = 128 * half + 16 * (u & 7) + 4 * (2 * (jt & 1) + (u >> 3)) + (jt >> 1);
    unsigned a0 = pack_bf16(acc[i][0][0], acc[i][0][1]), a1 = pack_bf16(acc[i][0][2], acc[i][0][3]);
    unsigned b0 = pack_bf16(acc[i][1][0], acc[i][1][1]), b1 = pack_bf16(acc[i][1][2], acc[i][1][3]);
    u32x2 r0 = __builtin_amdgcn_permlane16_swap(a0, b0, false, false);
    u32x2 r1 = __builtin_amdgcn_permlane16_swap(a1, b1, false, false);
    u32x4 v = {r0[0], r1[0], r0[1], r1[1]};
    if (tl < 250) *(u32x4*)(out + (size_t)(rb * 250 + tl) * 512 + col) = v;
  }
}

extern "C" void kernel_launch(void* const* d_in, const int* in_sizes, int n_in, void* d_out, int out_size,
                              void* d_ws, size_t ws_size, hipStream_t stream) {
  const u16* x = (const u16*)d_in[0];
  const u16* w1 = (const u16*)d_in[1];
  const u16* dw = (const u16*)d_in[2];
  const u16* w2 = (const u16*)d_in[3];
  (void)hipFuncSetAttribute((const void*)k1, hipFuncAttributeMaxDynamicSharedMemorySize, K1_LDS);
  (void)hipFuncSetAttribute((const void*)k2, hipFuncAttributeMaxDynamicSharedMemorySize, K2_LDS);
  k1<<<256, 512, K1_LDS, stream>>>(x, w1, dw, (char*)d_ws);
  k2<<<256, 512, K2_LDS, stream>>>((const char*)d_ws, w2, (u16*)d_out);
}
```

```cpp
#include <hip/hip_runtime.h>
#include <stdint.h>
#include <stdio.h>
#define PROF 1

typedef unsigned short u16;
typedef __attribute__((ext_vector_type(8))) short bf16x8;
typedef __attribute__((ext_vector_type(4))) short s16x4;
typedef __attribute__((ext_vector_type(4))) float f32x4;
typedef __attribute__((ext_vector_type(2))) float f32x2;
typedef __attribute__((ext_vector_type(2))) __bf16 bf16x2;
typedef __attribute__((ext_vector_type(4))) unsigned u32x4;
typedef __attribute__((ext_vector_type(2))) unsigned u32x2;
#define LDSP(T) __attribute__((address_space(3))) T*
#define GLBP(T) __attribute__((address_space(1))) T*

#define WAIT_V(n) asm volatile("s_waitcnt vmcnt(%0)" ::"n"(n) : "memory")
#define WAIT_L(n) asm volatile("s_waitcnt lgkmcnt(%0)" ::"n"(n) : "memory")
#define BAR() asm volatile("s_barrier" ::: "memory")
#define SCHED() __builtin_amdgcn_sched_barrier(0)

__device__ __forceinline__ unsigned pack_bf16(float a, float b) {
  f32x2 v = {a, b};
  return __builtin_bit_cast(unsigned, __builtin_convertvector(v, bf16x2));
}
__device__ __forceinline__ float sigmoid_mul(float a, float b) {
  return a * __builtin_amdgcn_rcpf(1.f + __builtin_amdgcn_exp2f(b * -1.44269504088896f));
}
__device__ __forceinline__ void glds16(const void* sbase, unsigned voff, unsigned lds) {
  asm volatile("s_mov_b32 m0, %0\n\ts_nop 0\n\tglobal_load_lds_dwordx4 %1, %2" ::"s"(lds), "v"(voff), "s"(sbase));
}
__device__ __forceinline__ unsigned lds_addr(const char* p) { return (unsigned)(size_t)(LDSP(const char))p; }
__device__ __forceinline__ bf16x8 lds_b128(const char* p) { return *(const bf16x8*)p; }
__device__ __forceinline__ bf16x8 lds_tr2(const char* p) {
  s16x4 lo = __builtin_amdgcn_ds_read_tr16_b64_v4i16((LDSP(s16x4))p);
  s16x4 hi = __builtin_amdgcn_ds_read_tr16_b64_v4i16((LDSP(s16x4))(p + 1024));
  return __builtin_shufflevector(lo, hi, 0, 1, 2, 3, 4, 5, 6, 7);
}

#define NTS 16
#define WL(val, i, old) (((int)(threadIdx.x & 63) == (int)(i)) ? (val) : (old))
#define TS_DECL unsigned ts_ = 0; ts_ = WL((unsigned)__builtin_amdgcn_s_memrealtime(), NTS - 2, ts_);
#define TS(i) do { SCHED(); ts_ = WL((unsigned)__builtin_amdgcn_s_memtime(), (i), ts_); SCHED(); } while (0)
#define TS_FLUSH(base, kidx) do { ts_ = WL((unsigned)__builtin_amdgcn_s_memrealtime(), NTS - 1, ts_); if ((threadIdx.x & 63) < NTS) { unsigned long long* p_ = (unsigned long long*)((char*)(base) + (64u << 20)) + ((size_t)(kidx) * 256 * 8 + blockIdx.x * 8 + (threadIdx.x >> 6)) * NTS; p_[threadIdx.x & 63] = ts_; } } while (0)
#define TS_ARG , unsigned& ts_
#define TS_PASS , ts_
constexpr int K1_XB = 272 * 128;
constexpr int K1_WB = 64 * 256;
constexpr int K1_ST = K1_XB + K1_WB;
constexpr int K1_TAB = 3 * K1_ST;
constexpr int K1_LDS = K1_TAB + 8192;
constexpr int HT_STRIDE = 560;

template <int NT, int NG>
__device__ __forceinline__ void k1_body(char* sm, const char* x, const char* w1, const unsigned (&xoff)[5],
                                        const unsigned (&woff)[2], int w, int l, int t0 TS_ARG) {
  const int g = l >> 4, m = l & 15;
  const int mt0 = (NT == 9) ? 0 : 9;
  auto issue = [&](int kt, int st) {
    const char* xs = x + kt * 128;
    const char* ws = w1 + kt * (64 * 2048);
    unsigned d = lds_addr(sm) + st;
#pragma unroll
    for (int i = 0; i < 2; i++) glds16(ws, woff[i], d + K1_XB + (2 * w + i) * 1024);
#pragma unroll
    for (int i = 0; i < 4; i++) glds16(xs, xoff[i], d + (w + 8 * i) * 1024);
    if (NG == 7) glds16(xs, xoff[4], d + (32 + w - 6) * 1024);
  };
  const int a_off0 = (mt0 * 16 + m) * 128 + ((g ^ (l & 7)) << 4);
  const int a_off1 = a_off0 ^ 64;
  const int q = m >> 2, p = m & 3, wn = w & 3;
  const int f = q | ((g & 1) << 2);
  const int b_offa = K1_XB + (8 * g + q) * 256 + ((wn ^ f) << 5) + p * 8;
  const int b_offb = b_offa ^ 128;

  f32x4 acca[NT], accb[NT];
#pragma unroll
  for (int i = 0; i < NT; i++) acca[i] = f32x4{0, 0, 0, 0}, accb[i] = f32x4{0, 0, 0, 0};
  bf16x8 A0[NT], A1[NT], Ba0, Bb0, Ba1, Bb1;

  TS(1);
  WAIT_V(2 * NG);
  BAR();
  TS(2);
  Ba0 = lds_tr2(sm + b_offa);
  Bb0 = lds_tr2(sm + b_offb);
#pragma unroll
  for (int i = 0; i < NT; i++) A0[i] = lds_b128(sm + a_off0 + i * 2048);
  int st = 0;
#pragma unroll 1
  for (int it = 0; it < 8; it++) {
    const char* sc = sm + st;
    Ba1 = lds_tr2(sc + b_offa + 8192);
    Bb1 = lds_tr2(sc + b_offb + 8192);
#pragma unroll
    for (int i = 0; i < NT; i++) A1[i] = lds_b128(sc + a_off1 + i * 2048);
#pragma unroll
    for (int i = 0; i < NT; i++) {
      acca[i] = __builtin_amdgcn_mfma_f32_16x16x32_bf16(A0[i], Ba0, acca[i], 0, 0, 0);
      accb[i] = __builtin_amdgcn_mfma_f32_16x16x32_bf16(A0[i], Bb0, accb[i], 0, 0, 0);
    }
#pragma unroll
    for (int i = 0; i < NT + 4; i++) {
      __builtin_amdgcn_sched_group_barrier(0x008, 1, 0);
      __builtin_amdgcn_sched_group_barrier(0x100, 1, 0);
    }
    __builtin_amdgcn_sched_group_barrier(0x008, NT - 4, 0);
    SCHED();
    int stn = (st == 2 * K1_ST) ? 0 : st + K1_ST;
    if (it < 7) {
      WAIT_L(0);
      if (it < 6) WAIT_V(NG);
      else WAIT_V(0);
      BAR();
      TS(3 + it);
      if (it < 5) issue(it + 3, st);
    }
    SCHED();
    const char* sn = sm + stn;
    Ba0 = lds_tr2(sn + b_offa);
    Bb0 = lds_tr2(sn + b_offb);
#pragma unroll
    for (int i = 0; i < NT; i++) A0[i] = lds_b128(sn + a_off0 + i * 2048);
#pragma unroll
    for (int i = 0; i < NT; i++) {
      acca[i] = __builtin_amdgcn_mfma_f32_16x16x32_bf16(A1[i], Ba1, acca[i], 0, 0, 0);
      accb[i] = __builtin_amdgcn_mfma_f32_16x16x32_bf16(A1[i], Bb1, accb[i], 0, 0, 0);
    }
#pragma unroll
    for (int i = 0; i < NT + 4; i++) {
      __builtin_amdgcn_sched_group_barrier(0x008, 1, 1);
      __builtin_amdgcn_sched_group_barrier(0x100, 1, 1);
    }
    __builtin_amdgcn_sched_group_barrier(0x008, NT - 4, 1);
    SCHED();
    st = stn;
  }
  TS(10);
  char* ht = sm + (wn * 16 + m) * HT_STRIDE + (mt0 * 16 + 4 * g) * 2;
  const int tb = t0 - 7 + mt0 * 16 + 4 * g;
#pragma unroll
  for (int i = 0; i < NT; i++) {
    float h[4];
#pragma unroll
    for (int r = 0; r < 4; r++) {
      float v = sigmoid_mul(acca[i][r], accb[i][r]);
      h[r] = ((unsigned)(tb + 16 * i + r) < 4000u) ? v : 0.f;
    }
    u32x2 pk = {pack_bf16(h[0], h[1]), pack_bf16(h[2], h[3])};
    *(u32x2*)(ht + i * 32) = pk;
  }
}

__global__ __launch_bounds__(512, 2) void k1(const u16* __restrict__ x, const u16* __restrict__ w1,
                                            const u16* __restrict__ dw, char* __restrict__ s) {
  extern __shared__ __attribute__((aligned(16))) char sm[];
  const int tid = threadIdx.x, l = tid & 63, w = __builtin_amdgcn_readfirstlane(tid >> 6);
  const int b = blockIdx.x, j = b >> 3, rb = (b & 7) * 4 + (j >> 3), cs = j & 7;
  const int batch = rb >> 4, t0 = (rb & 15) * 250;
  TS_DECL
  TS(0);

  unsigned tap[8];
  const int idx0 = (w & 3) * 8 - 8 + (w >> 2);
  {
    const unsigned voff = (cs * 64 + l) * 2;
#pragma unroll
    for (int e = 0; e < 8; e++) {
      int i = idx0 + e;
      const u16* p = dw + ((unsigned)i < 15u ? i : 0) * 512;
      asm volatile("global_load_ushort %0, %1, %2" : "=v"(tap[e]) : "v"(voff), "s"(p));
    }
  }
  unsigned xoff[5], woff[2];
#pragma unroll
  for (int i = 0; i < 5; i++) {
    int rowm = 8 * (i < 4 ? (w + 8 * i) : (32 + (w & 1))) + (l >> 3);
    int t = min(max(t0 - 7 + rowm, 0), 3999);
    xoff[i] = (unsigned)((batch * 4000 + t) * 1024 + (((l & 7) ^ (l >> 3)) << 4));
  }
#pragma unroll
  for (int i = 0; i < 2; i++) {
    int row = 8 * w + 4 * i + (l >> 4);
    int f = (l >> 4) | ((w & 1) << 2);
    int slot = ((l & 15) >> 1) ^ f;
    int col = (slot & 3) * 16 + (l & 1) * 8 + cs * 64 + (slot >> 2) * 512;
    woff[i] = (unsigned)((row * 1024 + col) * 2);
  }
  const char* xb = (const char*)x;
  const char* wb = (const char*)w1;
#pragma unroll
  for (int kt = 0; kt < 3; kt++) {
    const char* xs = xb + kt * 128;
    const char* ws = wb + kt * (64 * 2048);
    unsigned d = lds_addr(sm) + kt * K1_ST;
#pragma unroll
    for (int i = 0; i < 2; i++) glds16(ws, woff[i], d + K1_XB + (2 * w + i) * 1024);
#pragma unroll
    for (int i = 0; i < 4; i++) glds16(xs, xoff[i], d + (w + 8 * i) * 1024);
    if (w >= 6) glds16(xs, xoff[4], d + (32 + w - 6) * 1024);
  }
  {
    asm volatile("s_waitcnt vmcnt(18)"
                 : "+v"(tap[0]), "+v"(tap[1]), "+v"(tap[2]), "+v"(tap[3]), "+v"(tap[4]), "+v"(tap[5]), "+v"(tap[6]),
                   "+v"(tap[7]));
#pragma unroll
    for (int e = 0; e < 8; e++) tap[e] = ((unsigned)(idx0 + e) < 15u) ? tap[e] : 0u;
    u32x4 tv = {tap[0] | (tap[1] << 16), tap[2] | (tap[3] << 16), tap[4] | (tap[5] << 16), tap[6] | (tap[7] << 16)};
    *(u32x4*)(sm + K1_TAB + l * 128 + w * 16) = tv;
  }
  if (w < 4) k1_body<9, 6>(sm, xb, wb, xoff, woff, w, l, t0 TS_PASS);
  else if (w < 6) k1_body<8, 6>(sm, xb, wb, xoff, woff, w, l, t0 TS_PASS);
  else k1_body<8, 7>(sm, xb, wb, xoff, woff, w, l, t0 TS_PASS);
  TS(11);
  WAIT_L(0);
  BAR();
  TS(12);
  const int g = l >> 4, m = l & 15;
  int start = 8 * g - m;
  if (start < -8 || start > 14) start = -8;
  const char* tp = sm + K1_TAB + (8 * w) * 128 + (start & 1) * 64 + (((start + 8) >> 1) << 2);
  const char* hp = sm + (8 * w) * HT_STRIDE + 32 * m + 16 * g;
  f32x4 o[8];
#pragma unroll
  for (int c = 0; c < 8; c++) {
    const unsigned* t32 = (const unsigned*)(tp + c * 128);
    u32x4 av = {t32[0], t32[1], t32[2], t32[3]};
    bf16x8 hv = lds_b128(hp + c * HT_STRIDE);
    o[c] = __builtin_amdgcn_mfma_f32_16x16x32_bf16(__builtin_bit_cast(bf16x8, av), hv, f32x4{0, 0, 0, 0}, 0, 0, 0);
  }
  char* sp = s + (size_t)(((rb * 2 + (m >> 3)) * 64 + cs * 8 + w) * 2048 + g * 128 + (m & 7) * 16);
#pragma unroll
  for (int r = 0; r < 4; r++) {
    u32x4 v;
#pragma unroll
    for (int c = 0; c < 4; c++)
      v[c] = pack_bf16(sigmoid_mul(o[2 * c][r], o[2 * c][r]), sigmoid_mul(o[2 * c + 1][r], o[2 * c + 1][r]));
    *(u32x4*)(sp + r * 512) = v;
  }
  TS(13);
  TS_FLUSH(s, 0);
}

constexpr int K2_NS = 5;
constexpr int K2_ST = 32768;
constexpr int K2_LDS = K2_NS * K2_ST;

__global__ __launch_bounds__(512, 2) void k2(const char* __restrict__ s, const u16* __restrict__ w2,
                                            u16* __restrict__ out) {
  extern __shared__ __attribute__((aligned(16))) char sm[];
  const int tid = threadIdx.x, l = tid & 63, w = __builtin_amdgcn_readfirstlane(tid >> 6);
  const int b = blockIdx.x, j = b >> 3, rb = (b & 7) * 4 + (j >> 3), half = (j >> 2) & 1, ns = j & 3;
  TS_DECL
  TS(0);
  const char* sa = s + (size_t)(rb * 2 + half) * 131072 + w * 2048;
  const char* wb = (const char*)w2;
  unsigned woff[2];
#pragma unroll
  for (int i = 0; i < 2; i++) {
    int row = 8 * w + 4 * i + (l >> 4);
    int f = (l >> 4) | ((w & 1) << 2);
    int slot = ((l & 15) >> 1) ^ f;
    woff[i] = (unsigned)((row * 512 + ns * 128 + slot * 16 + (l & 1) * 8) * 2);
  }
  const unsigned aoff = l * 16;
  auto issue = [&](int kt) {
    unsigned d = lds_addr(sm) + (kt % K2_NS) * K2_ST;
#pragma unroll
    for (int i = 0; i < 2; i++) glds16(wb + kt * 65536, woff[i], d + 16384 + (2 * w + i) * 1024);
#pragma unroll
    for (int i = 0; i < 2; i++) glds16(sa + kt * 16384 + i * 1024, aoff, d + (2 * w + i) * 1024);
  };
#pragma unroll
  for (int kt = 0; kt < K2_NS; kt++) issue(kt);

  const int g = l >> 4, u = l & 15, q = u >> 2, p = u & 3, wm = w >> 2, wn = w & 3;
  const int f = q | ((g & 1) << 2);
  const int a_off = g * 2048 + wm * 1024 + u * 16;
  const int b_off0 = 16384 + (8 * g + q) * 256 + (((2 * wn) ^ f) << 5) + p * 8;
  const int b_off1 = b_off0 ^ 32;
  f32x4 acc[4][2];
#pragma unroll
  for (int i = 0; i < 4; i++) acc[i][0] = acc[i][1] = f32x4{0, 0, 0, 0};
  bf16x8 S0[4], S1[4], W00, W01, W10, W11;
  TS(1);
  WAIT_V(4 * (K2_NS - 1));
  BAR();
  TS(2);
  W00 = lds_tr2(sm + b_off0);
  W01 = lds_tr2(sm + b_off1);
#pragma unroll
  for (int i = 0; i < 4; i++) S0[i] = lds_b128(sm + a_off + i * 256);
#pragma unroll
  for (int it = 0; it < 8; it++) {
    const char* sc = sm + (it % K2_NS) * K2_ST;
    W10 = lds_tr2(sc + b_off0 + 8192);
    W11 = lds_tr2(sc + b_off1 + 8192);
#pragma unroll
    for (int i = 0; i < 4; i++) S1[i] = lds_b128(sc + a_off + 8192 + i * 256);
#pragma unroll
    for (int i = 0; i < 4; i++) {
      acc[i][0] = __builtin_amdgcn_mfma_f32_16x16x32_bf16(W00, S0[i], acc[i][0], 0, 0, 0);
      acc[i][1] = __builtin_amdgcn_mfma_f32_16x16x32_bf16(W01, S0[i], acc[i][1], 0, 0, 0);
    }
#pragma unroll
    for (int i = 0; i < 8; i++) {
      __builtin_amdgcn_sched_group_barrier(0x008, 1, 0);
      __builtin_amdgcn_sched_group_barrier(0x100, 1, 0);
    }
    SCHED();
    if (it < 7) {
      WAIT_L(0);
      constexpr int lastis = 7;
      int hi = it + K2_NS - 1 < lastis ? it + K2_NS - 1 : lastis;
      switch (hi - (it + 1)) {
        case 0: WAIT_V(0); break;
        case 1: WAIT_V(4); break;
        case 2: WAIT_V(8); break;
        case 3: WAIT_V(12); break;
        default: WAIT_V(16); break;
      }
      BAR();
      TS(3 + it);
      if (it + K2_NS < 8) issue(it + K2_NS);
      SCHED();
      const char* sn = sm + ((it + 1) % K2_NS) * K2_ST;
      W00 = lds_tr2(sn + b_off0);
      W01 = lds_tr2(sn + b_off1);
#pragma unroll
      for (int i = 0; i < 4; i++) S0[i] = lds_b128(sn + a_off + i * 256);
    }
#pragma unroll
    for (int i = 0; i < 4; i++) {
      acc[i][0] = __builtin_amdgcn_mfma_f32_16x16x32_bf16(W10, S1[i], acc[i][0], 0, 0, 0);
      acc[i][1] = __builtin_amdgcn_mfma_f32_16x16x32_bf16(W11, S1[i], acc[i][1], 0, 0, 0);
    }
#pragma unroll
    for (int i = 0; i < 8; i++) {
      __builtin_amdgcn_sched_group_barrier(0x008, 1, 1);
      __builtin_amdgcn_sched_group_barrier(0x100, 1, 1);
    }
    SCHED();
  }
  TS(10);
  const int col = ns * 128 + wn * 32 + (g & 1) * 16 + (g >> 1) * 8;
#pragma unroll
  for (int i = 0; i < 4; i++) {
    const int jt = 4 * wm + i;
    const int tl = 128 * half + 16 * (u & 7) + 4 * (2 * (jt & 1) + (u >> 3)) + (jt >> 1);
    unsigned a0 = pack_bf16(acc[i][0][0], acc[i][0][1]), a1 = pack_bf16(acc[i][0][2], acc[i][0][3]);
    unsigned b0 = pack_bf16(acc[i][1][0], acc[i][1][1]), b1 = pack_bf16(acc[i][1][2], acc[i][1][3]);
    u32x2 r0 = __builtin_amdgcn_permlane16_swap(a0, b0, false, false);
    u32x2 r1 = __builtin_amdgcn_permlane16_swap(a1, b1, false, false);
    u32x4 v = {r0[0], r1[0], r0[1], r1[1]};
    if (tl < 250) *(u32x4*)(out + (size_t)(rb * 250 + tl) * 512 + col) = v;
  }
  TS(13);
  TS_FLUSH(s, 1);
}

__global__ void k3(const char* ws) {
  const unsigned long long* h = (const unsigned long long*)(ws + (64u << 20));
  if (threadIdx.x != 0) return;
  unsigned rt = (unsigned)__builtin_amdgcn_s_memrealtime();
  if (((rt >> 2) & 15) != 0) return;
  unsigned smin[2], smax[2], emin[2], emax[2];
  unsigned sum[2][14];
  for (int k = 0; k < 2; k++) {
    smin[k] = ~0u; smax[k] = 0; emin[k] = ~0u; emax[k] = 0;
    for (int i = 0; i < 14; i++) sum[k][i] = 0;
    for (int b = 0; b < 256; b++) {
      for (int w = 0; w < 8; w++) {
        const unsigned long long* p = h + (size_t)(k * 256 * 8 + b * 8 + w) * NTS;
        unsigned s = (unsigned)p[NTS - 2], e = (unsigned)p[NTS - 1];
        smin[k] = min(smin[k], s); smax[k] = max(smax[k], s); emin[k] = min(emin[k], e); emax[k] = max(emax[k], e);
        if (w == 0) {
#pragma unroll
          for (int i = 1; i < 14; i++) sum[k][i] += (unsigned)p[i] - (unsigned)p[0];
        }
      }
    }
  }
  printf("K1 s %u..%u e %u..%u K2 s %u..%u e %u..%u k3 %u | K1 %u %u %u %u %u %u %u %u %u %u %u %u %u | K2 %u %u %u %u %u %u %u %u %u %u %u\n", 0u, smax[0] - smin[0], emin[0] - smin[0], emax[0] - smin[0],
         smin[1] - smin[0], smax[1] - smin[0], emin[1] - smin[0], emax[1] - smin[0], rt - smin[0],
         sum[0][1] >> 8, sum[0][2] >> 8, sum[0][3] >> 8, sum[0][4] >> 8, sum[0][5] >> 8, sum[0][6] >> 8, sum[0][7] >> 8, sum[0][8] >> 8, sum[0][9] >> 8, sum[0][10] >> 8, sum[0][11] >> 8, sum[0][12] >> 8, sum[0][13] >> 8,
         sum[1][1] >> 8, sum[1][2] >> 8, sum[1][3] >> 8, sum[1][4] >> 8, sum[1][5] >> 8, sum[1][6] >> 8, sum[1][7] >> 8, sum[1][8] >> 8, sum[1][9] >> 8, sum[1][10] >> 8, sum[1][13] >> 8);
}

extern "C" void kernel_launch(void* const* d_in, const int* in_sizes, int n_in, void* d_out, int out_size,
                              void* d_ws, size_t ws_size, hipStream_t stream) {
  const u16* x = (const u16*)d_in[0];
  const u16* w1 = (const u16*)d_in[1];
  const u16* dw = (const u16*)d_in[2];
  const u16* w2 = (const u16*)d_in[3];
  (void)hipFuncSetAttribute((const void*)k1, hipFuncAttributeMaxDynamicSharedMemorySize, K1_LDS);
  (void)hipFuncSetAttribute((const void*)k2, hipFuncAttributeMaxDynamicSharedMemorySize, K2_LDS);
  k1<<<256, 512, K1_LDS, stream>>>(x, w1, dw, (char*)d_ws);
  k2<<<256, 512, K2_LDS, stream>>>((const char*)d_ws, w2, (u16*)d_out);
  k3<<<1, 64, 0, stream>>>((const char*)d_ws);
}
```

```cpp
#include <hip/hip_runtime.h>
#include <stdint.h>
#define PROF 1

typedef unsigned short u16;
typedef __attribute__((ext_vector_type(8))) short bf16x8;
typedef __attribute__((ext_vector_type(4))) short s16x4;
typedef __attribute__((ext_vector_type(4))) float f32x4;
typedef __attribute__((ext_vector_type(2))) float f32x2;
typedef __attribute__((ext_vector_type(2))) __bf16 bf16x2;
typedef __attribute__((ext_vector_type(4))) unsigned u32x4;
typedef __attribute__((ext_vector_type(2))) unsigned u32x2;
#define LDSP(T) __attribute__((address_space(3))) T*
#define GLBP(T) __attribute__((address_space(1))) T*

#define WAIT_V(n) asm volatile("s_waitcnt vmcnt(%0)" ::"n"(n) : "memory")
#define WAIT_L(n) asm volatile("s_waitcnt lgkmcnt(%0)" ::"n"(n) : "memory")
#define BAR() asm volatile("s_barrier" ::: "memory")
#define SCHED() __builtin_amdgcn_sched_barrier(0)

__device__ __forceinline__ unsigned pack_bf16(float a, float b) {
  f32x2 v = {a, b};
  return __builtin_bit_cast(unsigned, __builtin_convertvector(v, bf16x2));
}
__device__ __forceinline__ float sigmoid_mul(float a, float b) {
  return a * __builtin_amdgcn_rcpf(1.f + __builtin_amdgcn_exp2f(b * -1.44269504088896f));
}
__device__ __forceinline__ void glds16(const void* sbase, unsigned voff, unsigned lds) {
  asm volatile("s_mov_b32 m0, %0\n\ts_nop 0\n\tglobal_load_lds_dwordx4 %1, %2" ::"s"(lds), "v"(voff), "s"(sbase));
}
__device__ __forceinline__ unsigned lds_addr(const char* p) { return (unsigned)(size_t)(LDSP(const char))p; }
__device__ __forceinline__ bf16x8 lds_b128(const char* p) { return *(const bf16x8*)p; }
__device__ __forceinline__ bf16x8 lds_tr2(const char* p) {
  s16x4 lo = __builtin_amdgcn_ds_read_tr16_b64_v4i16((LDSP(s16x4))p);
  s16x4 hi = __builtin_amdgcn_ds_read_tr16_b64_v4i16((LDSP(s16x4))(p + 1024));
  return __builtin_shufflevector(lo, hi, 0, 1, 2, 3, 4, 5, 6, 7);
}

#define NTS 16
#define WL(val, i, old) (((int)(threadIdx.x & 63) == (int)(i)) ? (val) : (old))
#define TS_DECL unsigned ts_ = 0; ts_ = WL((unsigned)__builtin_amdgcn_s_memrealtime(), NTS - 2, ts_);
#define TS(i) do { SCHED(); ts_ = WL((unsigned)__builtin_amdgcn_s_memtime(), (i), ts_); SCHED(); } while (0)
#define TS_FLUSH(base, kidx) do { ts_ = WL((unsigned)__builtin_amdgcn_s_memrealtime(), NTS - 1, ts_); if ((threadIdx.x & 63) < NTS) { unsigned long long* p_ = (unsigned long long*)((char*)(base) + (64u << 20)) + ((size_t)(kidx) * 256 * 8 + blockIdx.x * 8 + (threadIdx.x >> 6)) * NTS; p_[threadIdx.x & 63] = ts_; } } while (0)
#define TS_ARG , unsigned& ts_
#define TS_PASS , ts_
constexpr int K1_XB = 272 * 128;
constexpr int K1_WB = 64 * 256;
constexpr int K1_ST = K1_XB + K1_WB;
constexpr int K1_TAB = 3 * K1_ST;
constexpr int K1_LDS = K1_TAB + 8192;
constexpr int HT_STRIDE = 560;

template <int NT, int NG>
__device__ __forceinline__ void k1_body(char* sm, const char* x, const char* w1, const unsigned (&xoff)[5],
                                        const unsigned (&woff)[2], int w, int l, int t0 TS_ARG) {
  const int g = l >> 4, m = l & 15;
  const int mt0 = (NT == 9) ? 0 : 9;
  auto issue = [&](int kt, int st) {
    const char* xs = x + kt * 128;
    const char* ws = w1 + kt * (64 * 2048);
    unsigned d = lds_addr(sm) + st;
#pragma unroll
    for (int i = 0; i < 2; i++) glds16(ws, woff[i], d + K1_XB + (2 * w + i) * 1024);
#pragma unroll
    for (int i = 0; i < 4; i++) glds16(xs, xoff[i], d + (w + 8 * i) * 1024);
    if (NG == 7) glds16(xs, xoff[4], d + (32 + w - 6) * 1024);
  };
  const int a_off0 = (mt0 * 16 + m) * 128 + ((g ^ (l & 7)) << 4);
  const int a_off1 = a_off0 ^ 64;
  const int q = m >> 2, p = m & 3, wn = w & 3;
  const int f = q | ((g & 1) << 2);
  const int b_offa = K1_XB + (8 * g + q) * 256 + ((wn ^ f) << 5) + p * 8;
  const int b_offb = b_offa ^ 128;

  f32x4 acca[NT], accb[NT];
#pragma unroll
  for (int i = 0; i < NT; i++) acca[i] = f32x4{0, 0, 0, 0}, accb[i] = f32x4{0, 0, 0, 0};
  bf16x8 A0[NT], A1[NT], Ba0, Bb0, Ba1, Bb1;

  TS(1);
  WAIT_V(2 * NG);
  BAR();
  TS(2);
  Ba0 = lds_tr2(sm + b_offa);
  Bb0 = lds_tr2(sm + b_offb);
#pragma unroll
  for (int i = 0; i < NT; i++) A0[i] = lds_b128(sm + a_off0 + i * 2048);
  int st = 0;
#pragma unroll 1
  for (int it = 0; it < 8; it++) {
    const char* sc = sm + st;
    Ba1 = lds_tr2(sc + b_offa + 8192);
    Bb1 = lds_tr2(sc + b_offb + 8192);
#pragma unroll
    for (int i = 0; i < NT; i++) A1[i] = lds_b128(sc + a_off1 + i * 2048);
#pragma unroll
    for (int i = 0; i < NT; i++) {
      acca[i] = __builtin_amdgcn_mfma_f32_16x16x32_bf16(A0[i], Ba0, acca[i], 0, 0, 0);
      accb[i] = __builtin_amdgcn_mfma_f32_16x16x32_bf16(A0[i], Bb0, accb[i], 0, 0, 0);
    }
#pragma unroll
    for (int i = 0; i < NT + 4; i++) {
      __builtin_amdgcn_sched_group_barrier(0x008, 1, 0);
      __builtin_amdgcn_sched_group_barrier(0x100, 1, 0);
    }
    __builtin_amdgcn_sched_group_barrier(0x008, NT - 4, 0);
    SCHED();
    int stn = (st == 2 * K1_ST) ? 0 : st + K1_ST;
    if (it < 7) {
      WAIT_L(0);
      if (it < 6) WAIT_V(NG);
      else WAIT_V(0);
      BAR();
      TS(3 + it);
      if (it < 5) issue(it + 3, st);
    }
    SCHED();
    const char* sn = sm + stn;
    Ba0 = lds_tr2(sn + b_offa);
    Bb0 = lds_tr2(sn + b_offb);
#pragma unroll
    for (int i = 0; i < NT; i++) A0[i] = lds_b128(sn + a_off0 + i * 2048);
#pragma unroll
    for (int i = 0; i < NT; i++) {
      acca[i] = __builtin_amdgcn_mfma_f32_16x16x32_bf16(A1[i], Ba1, acca[i], 0, 0, 0);
      accb[i] = __builtin_amdgcn_mfma_f32_16x16x32_bf16(A1[i], Bb1, accb[i], 0, 0, 0);
    }
#pragma unroll
    for (int i = 0; i < NT + 4; i++) {
      __builtin_amdgcn_sched_group_barrier(0x008, 1, 1);
      __builtin_amdgcn_sched_group_barrier(0x100, 1, 1);
    }
    __builtin_amdgcn_sched_group_barrier(0x008, NT - 4, 1);
    SCHED();
    st = stn;
  }
  TS(10);
  char* ht = sm + (wn * 16 + m) * HT_STRIDE + (mt0 * 16 + 4 * g) * 2;
  const int tb = t0 - 7 + mt0 * 16 + 4 * g;
#pragma unroll
  for (int i = 0; i < NT; i++) {
    float h[4];
#pragma unroll
    for (int r = 0; r < 4; r++) {
      float v = sigmoid_mul(acca[i][r], accb[i][r]);
      h[r] = ((unsigned)(tb + 16 * i + r) < 4000u) ? v : 0.f;
    }
    u32x2 pk = {pack_bf16(h[0], h[1]), pack_bf16(h[2], h[3])};
    *(u32x2*)(ht + i * 32) = pk;
  }
}

__global__ __launch_bounds__(512, 2) void k1(const u16* __restrict__ x, const u16* __restrict__ w1,
                                            const u16* __restrict__ dw, char* __restrict__ s) {
  extern __shared__ __attribute__((aligned(16))) char sm[];
  const int tid = threadIdx.x, l = tid & 63, w = __builtin_amdgcn_readfirstlane(tid >> 6);
  const int b = blockIdx.x, j = b >> 3, rb = (b & 7) * 4 + (j >> 3), cs = j & 7;
  const int batch = rb >> 4, t0 = (rb & 15) * 250;
  TS_DECL
  TS(0);

  unsigned tap[8];
  const int idx0 = (w & 3) * 8 - 8 + (w >> 2);
  {
    const unsigned voff = (cs * 64 + l) * 2;
#pragma unroll
    for (int e = 0; e < 8; e++) {
      int i = idx0 + e;
      const u16* p = dw + ((unsigned)i < 15u ? i : 0) * 512;
      asm volatile("global_load_ushort %0, %1, %2" : "=v"(tap[e]) : "v"(voff), "s"(p));
    }
  }
  unsigned xoff[5], woff[2];
#pragma unroll
  for (int i = 0; i < 5; i++) {
    int rowm = 8 * (i < 4 ? (w + 8 * i) : (32 + (w & 1))) + (l >> 3);
    int t = min(max(t0 - 7 + rowm, 0), 3999);
    xoff[i] = (unsigned)((batch * 4000 + t) * 1024 + (((l & 7) ^ (l >> 3)) << 4));
  }
#pragma unroll
  for (int i = 0; i < 2; i++) {
    int row = 8 * w + 4 * i + (l >> 4);
    int f = (l >> 4) | ((w & 1) << 2);
    int slot = ((l & 15) >> 1) ^ f;
    int col = (slot & 3) * 16 + (l & 1) * 8 + cs * 64 + (slot >> 2) * 512;
    woff[i] = (unsigned)((row * 1024 + col) * 2);
  }
  const char* xb = (const char*)x;
  const char* wb = (const char*)w1;
#pragma unroll
  for (int kt = 0; kt < 3; kt++) {
    const char* xs = xb + kt * 128;
    const char* ws = wb + kt * (64 * 2048);
    unsigned d = lds_addr(sm) + kt * K1_ST;
#pragma unroll
    for (int i = 0; i < 2; i++) glds16(ws, woff[i], d + K1_XB + (2 * w + i) * 1024);
#pragma unroll
    for (int i = 0; i < 4; i++) glds16(xs, xoff[i], d + (w + 8 * i) * 1024);
    if (w >= 6) glds16(xs, xoff[4], d + (32 + w - 6) * 1024);
  }
  {
    asm volatile("s_waitcnt vmcnt(18)"
                 : "+v"(tap[0]), "+v"(tap[1]), "+v"(tap[2]), "+v"(tap[3]), "+v"(tap[4]), "+v"(tap[5]), "+v"(tap[6]),
                   "+v"(tap[7]));
#pragma unroll
    for (int e = 0; e < 8; e++) tap[e] = ((unsigned)(idx0 + e) < 15u) ? tap[e] : 0u;
    u32x4 tv = {tap[0] | (tap[1] << 16), tap[2] | (tap[3] << 16), tap[4] | (tap[5] << 16), tap[6] | (tap[7] << 16)};
    *(u32x4*)(sm + K1_TAB + l * 128 + w * 16) = tv;
  }
  if (w < 4) k1_body<9, 6>(sm, xb, wb, xoff, woff, w, l, t0 TS_PASS);
  else if (w < 6) k1_body<8, 6>(sm, xb, wb, xoff, woff, w, l, t0 TS_PASS);
  else k1_body<8, 7>(sm, xb, wb, xoff, woff, w, l, t0 TS_PASS);
  TS(11);
  WAIT_L(0);
  BAR();
  TS(12);
  const int g = l >> 4, m = l & 15;
  int start = 8 * g - m;
  if (start < -8 || start > 14) start = -8;
  const char* tp = sm + K1_TAB + (8 * w) * 128 + (start & 1) * 64 + (((start + 8) >> 1) << 2);
  const char* hp = sm + (8 * w) * HT_STRIDE + 32 * m + 16 * g;
  f32x4 o[8];
#pragma unroll
  for (int c = 0; c < 8; c++) {
    const unsigned* t32 = (const unsigned*)(tp + c * 128);
    u32x4 av = {t32[0], t32[1], t32[2], t32[3]};
    bf16x8 hv = lds_b128(hp + c * HT_STRIDE);
    o[c] = __builtin_amdgcn_mfma_f32_16x16x32_bf16(__builtin_bit_cast(bf16x8, av), hv, f32x4{0, 0, 0, 0}, 0, 0, 0);
  }
  char* sp = s + (size_t)(((rb * 2 + (m >> 3)) * 64 + cs * 8 + w) * 2048 + g * 128 + (m & 7) * 16);
#pragma unroll
  for (int r = 0; r < 4; r++) {
    u32x4 v;
#pragma unroll
    for (int c = 0; c < 4; c++)
      v[c] = pack_bf16(sigmoid_mul(o[2 * c][r], o[2 * c][r]), sigmoid_mul(o[2 * c + 1][r], o[2 * c + 1][r]));
    *(u32x4*)(sp + r * 512) = v;
  }
  TS(13);
  TS_FLUSH(s, 0);
}

constexpr int K2_NS = 5;
constexpr int K2_ST = 32768;
constexpr int K2_LDS = K2_NS * K2_ST;

__global__ __launch_bounds__(512, 2) void k2(const char* __restrict__ s, const u16* __restrict__ w2,
                                            u16* __restrict__ out) {
  extern __shared__ __attribute__((aligned(16))) char sm[];
  const int tid = threadIdx.x, l = tid & 63, w = __builtin_amdgcn_readfirstlane(tid >> 6);
  const int b = blockIdx.x, j = b >> 3, rb = (b & 7) * 4 + (j >> 3), half = (j >> 2) & 1, ns = j & 3;
  TS_DECL
  TS(0);
  const char* sa = s + (size_t)(rb * 2 + half) * 131072 + w * 2048;
  const char* wb = (const char*)w2;
  unsigned woff[2];
#pragma unroll
  for (int i = 0; i < 2; i++) {
    int row = 8 * w + 4 * i + (l >> 4);
    int f = (l >> 4) | ((w & 1) << 2);
    int slot = ((l & 15) >> 1) ^ f;
    woff[i] = (unsigned)((row * 512 + ns * 128 + slot * 16 + (l & 1) * 8) * 2);
  }
  const unsigned aoff = l * 16;
  auto issue = [&](int kt) {
    unsigned d = lds_addr(sm) + (kt % K2_NS) * K2_ST;
#pragma unroll
    for (int i = 0; i < 2; i++) glds16(wb + kt * 65536, woff[i], d + 16384 + (2 * w + i) * 1024);
#pragma unroll
    for (int i = 0; i < 2; i++) glds16(sa + kt * 16384 + i * 1024, aoff, d + (2 * w + i) * 1024);
  };
#pragma unroll
  for (int kt = 0; kt < K2_NS; kt++) issue(kt);

  const int g = l >> 4, u = l & 15, q = u >> 2, p = u & 3, wm = w >> 2, wn = w & 3;
  const int f = q | ((g & 1) << 2);
  const int a_off = g * 2048 + wm * 1024 + u * 16;
  const int b_off0 = 16384 + (8 * g + q) * 256 + (((2 * wn) ^ f) << 5) + p * 8;
  const int b_off1 = b_off0 ^ 32;
  f32x4 acc[4][2];
#pragma unroll
  for (int i = 0; i < 4; i++) acc[i][0] = acc[i][1] = f32x4{0, 0, 0, 0};
  bf16x8 S0[4], S1[4], W00, W01, W10, W11;
  TS(1);
  WAIT_V(4 * (K2_NS - 1));
  BAR();
  TS(2);
  W00 = lds_tr2(sm + b_off0);
  W01 = lds_tr2(sm + b_off1);
#pragma unroll
  for (int i = 0; i < 4; i++) S0[i] = lds_b128(sm + a_off + i * 256);
#pragma unroll
  for (int it = 0; it < 8; it++) {
    const char* sc = sm + (it % K2_NS) * K2_ST;
    W10 = lds_tr2(sc + b_off0 + 8192);
    W11 = lds_tr2(sc + b_off1 + 8192);
#pragma unroll
    for (int i = 0; i < 4; i++) S1[i] = lds_b128(sc + a_off + 8192 + i * 256);
#pragma unroll
    for (int i = 0; i < 4; i++) {
      acc[i][0] = __builtin_amdgcn_mfma_f32_16x16x32_bf16(W00, S0[i], acc[i][0], 0, 0, 0);
      acc[i][1] = __builtin_amdgcn_mfma_f32_16x16x32_bf16(W01, S0[i], acc[i][1], 0, 0, 0);
    }
#pragma unroll
    for (int i = 0; i < 8; i++) {
      __builtin_amdgcn_sched_group_barrier(0x008, 1, 0);
      __builtin_amdgcn_sched_group_barrier(0x100, 1, 0);
    }
    SCHED();
    if (it < 7) {
      WAIT_L(0);
      constexpr int lastis = 7;
      int hi = it + K2_NS - 1 < lastis ? it + K2_NS - 1 : lastis;
      switch (hi - (it + 1)) {
        case 0: WAIT_V(0); break;
        case 1: WAIT_V(4); break;
        case 2: WAIT_V(8); break;
        case 3: WAIT_V(12); break;
        default: WAIT_V(16); break;
      }
      BAR();
      TS(3 + it);
      if (it + K2_NS < 8) issue(it + K2_NS);
      SCHED();
      const char* sn = sm + ((it + 1) % K2_NS) * K2_ST;
      W00 = lds_tr2(sn + b_off0);
      W01 = lds_tr2(sn + b_off1);
#pragma unroll
      for (int i = 0; i < 4; i++) S0[i] = lds_b128(sn + a_off + i * 256);
    }
#pragma unroll
    for (int i = 0; i < 4; i++) {
      acc[i][0] = __builtin_amdgcn_mfma_f32_16x16x32_bf16(W10, S1[i], acc[i][0], 0, 0, 0);
      acc[i][1] = __builtin_amdgcn_mfma_f32_16x16x32_bf16(W11, S1[i], acc[i][1], 0, 0, 0);
    }
#pragma unroll
    for (int i = 0; i < 8; i++) {
      __builtin_amdgcn_sched_group_barrier(0x008, 1, 1);
      __builtin_amdgcn_sched_group_barrier(0x100, 1, 1);
    }
    SCHED();
  }
  TS(10);
  const int col = ns * 128 + wn * 32 + (g & 1) * 16 + (g >> 1) * 8;
#pragma unroll
  for (int i = 0; i < 4; i++) {
    const int jt = 4 * wm + i;
    const int tl = 128 * half + 16 * (u & 7) + 4 * (2 * (jt & 1) + (u >> 3)) + (jt >> 1);
    unsigned a0 = pack_bf16(acc[i][0][0], acc[i][0][1]), a1 = pack_bf16(acc[i][0][2], acc[i][0][3]);
    unsigned b0 = pack_bf16(acc[i][1][0], acc[i][1][1]), b1 = pack_bf16(acc[i][1][2], acc[i][1][3]);
    u32x2 r0 = __builtin_amdgcn_permlane16_swap(a0, b0, false, false);
    u32x2 r1 = __builtin_amdgcn_permlane16_swap(a1, b1, false, false);
    u32x4 v = {r0[0], r1[0], r0[1], r1[1]};
    if (tl < 250) *(u32x4*)(out + (size_t)(rb * 250 + tl) * 512 + col) = v;
  }
  TS(13);
  TS_FLUSH(s, 1);
}

__global__ void k3(char* ws) {
  const unsigned long long* h = (const unsigned long long*)(ws + (64u << 20));
  __shared__ unsigned smin[2], smax[2], emin[2], emax[2], sum[2][16], mx[2][16];
  const int b = threadIdx.x;
  if (b < 2) { smin[b] = ~0u; smax[b] = 0; emin[b] = ~0u; emax[b] = 0; }
  if (b < 32) { sum[b >> 4][b & 15] = 0; mx[b >> 4][b & 15] = 0; }
  __syncthreads();
  for (int k = 0; k < 2; k++) {
    for (int w = 0; w < 8; w++) {
      const unsigned long long* p = h + (size_t)(k * 256 * 8 + b * 8 + w) * NTS;
      unsigned s = (unsigned)p[NTS - 2], e = (unsigned)p[NTS - 1];
      atomicMin(&smin[k], s); atomicMax(&smax[k], s); atomicMin(&emin[k], e); atomicMax(&emax[k], e);
      if (w == 0) {
#pragma unroll
        for (int i = 1; i < 14; i++) { unsigned d = (unsigned)p[i] - (unsigned)p[0]; atomicAdd(&sum[k][i], d); atomicMax(&mx[k][i], d); }
      }
    }
  }
  __syncthreads();
  if (b == 0) {
#define C(k, i) ((sum[k][i] >> 8) / 10)
#define M(k, i) ((mx[k][i]) / 10)
    unsigned v = (smax[0]-smin[0]);
    if (v > 20000u) v = 20000u;
    unsigned target = 6000u + v;
    while ((unsigned)__builtin_amdgcn_s_memrealtime() - smin[0] < target) __builtin_amdgcn_s_sleep(1);
  }
}

extern "C" void kernel_launch(void* const* d_in, const int* in_sizes, int n_in, void* d_out, int out_size,
                              void* d_ws, size_t ws_size, hipStream_t stream) {
  const u16* x = (const u16*)d_in[0];
  const u16* w1 = (const u16*)d_in[1];
  const u16* dw = (const u16*)d_in[2];
  const u16* w2 = (const u16*)d_in[3];
  (void)hipFuncSetAttribute((const void*)k1, hipFuncAttributeMaxDynamicSharedMemorySize, K1_LDS);
  (void)hipFuncSetAttribute((const void*)k2, hipFuncAttributeMaxDynamicSharedMemorySize, K2_LDS);
  k1<<<256, 512, K1_LDS, stream>>>(x, w1, dw, (char*)d_ws);
  k2<<<256, 512, K2_LDS, stream>>>((const char*)d_ws, w2, (u16*)d_out);
  k3<<<1, 256, 0, stream>>>((char*)d_ws);
}
```

```cpp
#include <hip/hip_runtime.h>
#include <stdint.h>

typedef unsigned short u16;
typedef __attribute__((ext_vector_type(8))) short bf16x8;
typedef __attribute__((ext_vector_type(4))) short s16x4;
typedef __attribute__((ext_vector_type(4))) float f32x4;
typedef __attribute__((ext_vector_type(2))) float f32x2;
typedef __attribute__((ext_vector_type(2))) __bf16 bf16x2;
typedef __attribute__((ext_vector_type(4))) unsigned u32x4;
typedef __attribute__((ext_vector_type(2))) unsigned u32x2;
#define LDSP(T) __attribute__((address_space(3))) T*
#define GLBP(T) __attribute__((address_space(1))) T*

#define WAIT_V(n) asm volatile("s_waitcnt vmcnt(%0)" ::"n"(n) : "memory")
#define WAIT_L(n) asm volatile("s_waitcnt lgkmcnt(%0)" ::"n"(n) : "memory")
#define BAR() asm volatile("s_barrier" ::: "memory")
#define SCHED() __builtin_amdgcn_sched_barrier(0)

__device__ __forceinline__ unsigned pack_bf16(float a, float b) {
  f32x2 v = {a, b};
  return __builtin_bit_cast(unsigned, __builtin_convertvector(v, bf16x2));
}
__device__ __forceinline__ float sigmoid_mul(float a, float b) {
  return a * __builtin_amdgcn_rcpf(1.f + __builtin_amdgcn_exp2f(b * -1.44269504088896f));
}
__device__ __forceinline__ void glds16(const void* sbase, unsigned voff, unsigned lds) {
  asm volatile("s_mov_b32 m0, %0\n\ts_nop 0\n\tglobal_load_lds_dwordx4 %1, %2" ::"s"(lds), "v"(voff), "s"(sbase));
}
__device__ __forceinline__ unsigned lds_addr(const char* p) { return (unsigned)(size_t)(LDSP(const char))p; }
__device__ __forceinline__ bf16x8 lds_b128(const char* p) { return *(const bf16x8*)p; }
__device__ __forceinline__ bf16x8 lds_tr2(const char* p) {
  s16x4 lo = __builtin_amdgcn_ds_read_tr16_b64_v4i16((LDSP(s16x4))p);
  s16x4 hi = __builtin_amdgcn_ds_read_tr16_b64_v4i16((LDSP(s16x4))(p + 1024));
  return __builtin_shufflevector(lo, hi, 0, 1, 2, 3, 4, 5, 6, 7);
}

constexpr int K1_XB = 272 * 128;
constexpr int K1_WB = 64 * 256;
constexpr int K1_ST = K1_XB + K1_WB;
constexpr int K1_TAB = 3 * K1_ST;
constexpr int K1_LDS = K1_TAB + 8192;
constexpr int HT_STRIDE = 560;

template <int NT, int NG>
__device__ __forceinline__ void k1_body(char* sm, const char* x, const char* w1, const unsigned (&xoff)[5],
                                        const unsigned (&woff)[2], int w, int l, int t0) {
  const int g = l >> 4, m = l & 15;
  const int mt0 = (NT == 9) ? 0 : 9;
  auto issue = [&](int kt, int st) {
    const char* xs = x + kt * 128;
    const char* ws = w1 + kt * (64 * 2048);
    unsigned d = lds_addr(sm) + st;
#pragma unroll
    for (int i = 0; i < 2; i++) glds16(ws, woff[i], d + K1_XB + (2 * w + i) * 1024);
#pragma unroll
    for (int i = 0; i < 4; i++) glds16(xs, xoff[i], d + (w + 8 * i) * 1024);
    if (NG == 7) glds16(xs, xoff[4], d + (32 + w - 6) * 1024);
  };
  const int a_off0 = (mt0 * 16 + m) * 128 + ((g ^ (l & 7)) << 4);
  const int a_off1 = a_off0 ^ 64;
  const int q = m >> 2, p = m & 3, wn = w & 3;
  const int f = q | ((g & 1) << 2);
  const int b_offa = K1_XB + (8 * g + q) * 256 + ((wn ^ f) << 5) + p * 8;
  const int b_offb = b_offa ^ 128;

  f32x4 acca[NT], accb[NT];
#pragma unroll
  for (int i = 0; i < NT; i++) acca[i] = f32x4{0, 0, 0, 0}, accb[i] = f32x4{0, 0, 0, 0};
  bf16x8 A0[NT], A1[NT], Ba0, Bb0, Ba1, Bb1;

  WAIT_V(2 * NG);
  BAR();
  Ba0 = lds_tr2(sm + b_offa);
  Bb0 = lds_tr2(sm + b_offb);
#pragma unroll
  for (int i = 0; i < NT; i++) A0[i] = lds_b128(sm + a_off0 + i * 2048);
  int st = 0;
#pragma unroll 1
  for (int it = 0; it < 8; it++) {
    const char* sc = sm + st;
    Ba1 = lds_tr2(sc + b_offa + 8192);
    Bb1 = lds_tr2(sc + b_offb + 8192);
#pragma unroll
    for (int i = 0; i < NT; i++) A1[i] = lds_b128(sc + a_off1 + i * 2048);
#pragma unroll
    for (int i = 0; i < NT; i++) {
      acca[i] = __builtin_amdgcn_mfma_f32_16x16x32_bf16(A0[i], Ba0, acca[i], 0, 0, 0);
      accb[i] = __builtin_amdgcn_mfma_f32_16x16x32_bf16(A0[i], Bb0, accb[i], 0, 0, 0);
    }
#pragma unroll
    for (int i = 0; i < NT + 4; i++) {
      __builtin_amdgcn_sched_group_barrier(0x008, 1, 0);
      __builtin_amdgcn_sched_group_barrier(0x100, 1, 0);
    }
    __builtin_amdgcn_sched_group_barrier(0x008, NT - 4, 0);
    SCHED();
    int stn = (st == 2 * K1_ST) ? 0 : st + K1_ST;
    if (it < 7) {
      WAIT_L(0);
      if (it < 6) WAIT_V(NG);
      else WAIT_V(0);
      BAR();
      if (it < 5) issue(it + 3, st);
    }
    SCHED();
    const char* sn = sm + stn;
    Ba0 = lds_tr2(sn + b_offa);
    Bb0 = lds_tr2(sn + b_offb);
#pragma unroll
    for (int i = 0; i < NT; i++) A0[i] = lds_b128(sn + a_off0 + i * 2048);
#pragma unroll
    for (int i = 0; i < NT; i++) {
      acca[i] = __builtin_amdgcn_mfma_f32_16x16x32_bf16(A1[i], Ba1, acca[i], 0, 0, 0);
      accb[i] = __builtin_amdgcn_mfma_f32_16x16x32_bf16(A1[i], Bb1, accb[i], 0, 0, 0);
    }
#pragma unroll
    for (int i = 0; i < NT + 4; i++) {
      __builtin_amdgcn_sched_group_barrier(0x008, 1, 1);
      __builtin_amdgcn_sched_group_barrier(0x100, 1, 1);
    }
    __builtin_amdgcn_sched_group_barrier(0x008, NT - 4, 1);
    SCHED();
    st = stn;
  }
  char* ht = sm + (wn * 16 + m) * HT_STRIDE + (mt0 * 16 + 4 * g) * 2;
  const int tb = t0 - 7 + mt0 * 16 + 4 * g;
#pragma unroll
  for (int i = 0; i < NT; i++) {
    float h[4];
#pragma unroll
    for (int r = 0; r < 4; r++) {
      float v = sigmoid_mul(acca[i][r], accb[i][r]);
      h[r] = ((unsigned)(tb + 16 * i + r) < 4000u) ? v : 0.f;
    }
    u32x2 pk = {pack_bf16(h[0], h[1]), pack_bf16(h[2], h[3])};
    *(u32x2*)(ht + i * 32) = pk;
  }
}

__global__ __launch_bounds__(512, 2) void k1(const u16* __restrict__ x, const u16* __restrict__ w1,
                                            const u16* __restrict__ dw, char* __restrict__ s) {
  extern __shared__ __attribute__((aligned(16))) char sm[];
  const int tid = threadIdx.x, l = tid & 63, w = __builtin_amdgcn_readfirstlane(tid >> 6);
  const int b = blockIdx.x, j = b >> 3, rb = (b & 7) * 4 + (j >> 3), cs = j & 7;
  const int batch = rb >> 4, t0 = (rb & 15) * 250;

  unsigned tap[8];
  const int idx0 = (w & 3) * 8 - 8 + (w >> 2);
  {
    const unsigned voff = (cs * 64 + l) * 2;
#pragma unroll
    for (int e = 0; e < 8; e++) {
      int i = idx0 + e;
      const u16* p = dw + ((unsigned)i < 15u ? i : 0) * 512;
      asm volatile("global_load_ushort %0, %1, %2" : "=v"(tap[e]) : "v"(voff), "s"(p));
    }
  }
  unsigned xoff[5], woff[2];
#pragma unroll
  for (int i = 0; i < 5; i++) {
    int rowm = 8 * (i < 4 ? (w + 8 * i) : (32 + (w & 1))) + (l >> 3);
    int t = min(max(t0 - 7 + rowm, 0), 3999);
    xoff[i] = (unsigned)((batch * 4000 + t) * 1024 + (((l & 7) ^ (l >> 3)) << 4));
  }
#pragma unroll
  for (int i = 0; i < 2; i++) {
    int row = 8 * w + 4 * i + (l >> 4);
    int f = (l >> 4) | ((w & 1) << 2);
    int slot = ((l & 15) >> 1) ^ f;
    int col = (slot & 3) * 16 + (l & 1) * 8 + cs * 64 + (slot >> 2) * 512;
    woff[i] = (unsigned)((row * 1024 + col) * 2);
  }
  const char* xb = (const char*)x;
  const char* wb = (const char*)w1;
#pragma unroll
  for (int kt = 0; kt < 3; kt++) {
    const char* xs = xb + kt * 128;
    const char* ws = wb + kt * (64 * 2048);
    unsigned d = lds_addr(sm) + kt * K1_ST;
#pragma unroll
    for (int i = 0; i < 2; i++) glds16(ws, woff[i], d + K1_XB + (2 * w + i) * 1024);
#pragma unroll
    for (int i = 0; i < 4; i++) glds16(xs, xoff[i], d + (w + 8 * i) * 1024);
    if (w >= 6) glds16(xs, xoff[4], d + (32 + w - 6) * 1024);
  }
  {
    asm volatile("s_waitcnt vmcnt(18)"
                 : "+v"(tap[0]), "+v"(tap[1]), "+v"(tap[2]), "+v"(tap[3]), "+v"(tap[4]), "+v"(tap[5]), "+v"(tap[6]),
                   "+v"(tap[7]));
#pragma unroll
    for (int e = 0; e < 8; e++) tap[e] = ((unsigned)(idx0 + e) < 15u) ? tap[e] : 0u;
    u32x4 tv = {tap[0] | (tap[1] << 16), tap[2] | (tap[3] << 16), tap[4] | (tap[5] << 16), tap[6] | (tap[7] << 16)};
    *(u32x4*)(sm + K1_TAB + l * 128 + w * 16) = tv;
  }
  if (w < 4) k1_body<9, 6>(sm, xb, wb, xoff, woff, w, l, t0);
  else if (w < 6) k1_body<8, 6>(sm, xb, wb, xoff, woff, w, l, t0);
  else k1_body<8, 7>(sm, xb, wb, xoff, woff, w, l, t0);
  WAIT_L(0);
  BAR();
  const int g = l >> 4, m = l & 15;
  int start = 8 * g - m;
  if (start < -8 || start > 14) start = -8;
  const char* tp = sm + K1_TAB + (8 * w) * 128 + (start & 1) * 64 + (((start + 8) >> 1) << 2);
  const char* hp = sm + (8 * w) * HT_STRIDE + 32 * m + 16 * g;
  f32x4 o[8];
#pragma unroll
  for (int c = 0; c < 8; c++) {
    const unsigned* t32 = (const unsigned*)(tp + c * 128);
    u32x4 av = {t32[0], t32[1], t32[2], t32[3]};
    bf16x8 hv = lds_b128(hp + c * HT_STRIDE);
    o[c] = __builtin_amdgcn_mfma_f32_16x16x32_bf16(__builtin_bit_cast(bf16x8, av), hv, f32x4{0, 0, 0, 0}, 0, 0, 0);
  }
  char* sp = s + (size_t)(((rb * 2 + (m >> 3)) * 64 + cs * 8 + w) * 2048 + g * 128 + (m & 7) * 16);
#pragma unroll
  for (int r = 0; r < 4; r++) {
    u32x4 v;
#pragma unroll
    for (int c = 0; c < 4; c++)
      v[c] = pack_bf16(sigmoid_mul(o[2 * c][r], o[2 * c][r]), sigmoid_mul(o[2 * c + 1][r], o[2 * c + 1][r]));
    *(u32x4*)(sp + r * 512) = v;
  }
}

constexpr int K2_NS = 5;
constexpr int K2_ST = 32768;
constexpr int K2_LDS = K2_NS * K2_ST;

__global__ __launch_bounds__(512, 2) void k2(const char* __restrict__ s, const u16* __restrict__ w2,
                                            u16* __restrict__ out) {
  extern __shared__ __attribute__((aligned(16))) char sm[];
  const int tid = threadIdx.x, l = tid & 63, w = __builtin_amdgcn_readfirstlane(tid >> 6);
  const int b = blockIdx.x, j = b >> 3, rb = (b & 7) * 4 + (j >> 3), half = (j >> 2) & 1, ns = j & 3;
  const char* sa = s + (size_t)(rb * 2 + half) * 131072 + w * 2048;
  const char* wb = (const char*)w2;
  unsigned woff[2];
#pragma unroll
  for (int i = 0; i < 2; i++) {
    int row = 8 * w + 4 * i + (l >> 4);
    int f = (l >> 4) | ((w & 1) << 2);
    int slot = ((l & 15) >> 1) ^ f;
    woff[i] = (unsigned)((row * 512 + ns * 128 + slot * 16 + (l & 1) * 8) * 2);
  }
  const unsigned aoff = l * 16;
  auto issue = [&](int kt) {
    unsigned d = lds_addr(sm) + (kt % K2_NS) * K2_ST;
#pragma unroll
    for (int i = 0; i < 2; i++) glds16(wb + kt * 65536, woff[i], d + 16384 + (2 * w + i) * 1024);
#pragma unroll
    for (int i = 0; i < 2; i++) glds16(sa + kt * 16384 + i * 1024, aoff, d + (2 * w + i) * 1024);
  };
#pragma unroll
  for (int kt = 0; kt < K2_NS; kt++) issue(kt);

  const int g = l >> 4, u = l & 15, q = u >> 2, p = u & 3, wm = w >> 2, wn = w & 3;
  const int f = q | ((g & 1) << 2);
  const int a_off = g * 2048 + wm * 1024 + u * 16;
  const int b_off0 = 16384 + (8 * g + q) * 256 + (((2 * wn) ^ f) << 5) + p * 8;
  const int b_off1 = b_off0 ^ 32;
  f32x4 acc[4][2];
#pragma unroll
  for (int i = 0; i < 4; i++) acc[i][0] = acc[i][1] = f32x4{0, 0, 0, 0};
  bf16x8 S0[4], S1[4], W00, W01, W10, W11;
  WAIT_V(4 * (K2_NS - 1));
  BAR();
  W00 = lds_tr2(sm + b_off0);
  W01 = lds_tr2(sm + b_off1);
#pragma unroll
  for (int i = 0; i < 4; i++) S0[i] = lds_b128(sm + a_off + i * 256);
#pragma unroll
  for (int it = 0; it < 8; it++) {
    const char* sc = sm + (it % K2_NS) * K2_ST;
    W10 = lds_tr2(sc + b_off0 + 8192);
    W11 = lds_tr2(sc + b_off1 + 8192);
#pragma unroll
    for (int i = 0; i < 4; i++) S1[i] = lds_b128(sc + a_off + 8192 + i * 256);
#pragma unroll
    for (int i = 0; i < 4; i++) {
      acc[i][0] = __builtin_amdgcn_mfma_f32_16x16x32_bf16(W00, S0[i], acc[i][0], 0, 0, 0);
      acc[i][1] = __builtin_amdgcn_mfma_f32_16x16x32_bf16(W01, S0[i], acc[i][1], 0, 0, 0);
    }
#pragma unroll
    for (int i = 0; i < 8; i++) {
      __builtin_amdgcn_sched_group_barrier(0x008, 1, 0);
      __builtin_amdgcn_sched_group_barrier(0x100, 1, 0);
    }
    SCHED();
    if (it < 7) {
      WAIT_L(0);
      constexpr int lastis = 7;
      int hi = it + K2_NS - 1 < lastis ? it + K2_NS - 1 : lastis;
      switch (hi - (it + 1)) {
        case 0: WAIT_V(0); break;
        case 1: WAIT_V(4); break;
        case 2: WAIT_V(8); break;
        case 3: WAIT_V(12); break;
        default: WAIT_V(16); break;
      }
      BAR();
      if (it + K2_NS < 8) issue(it + K2_NS);
      SCHED();
      const char* sn = sm + ((it + 1) % K2_NS) * K2_ST;
      W00 = lds_tr2(sn + b_off0);
      W01 = lds_tr2(sn + b_off1);
#pragma unroll
      for (int i = 0; i < 4; i++) S0[i] = lds_b128(sn + a_off + i * 256);
    }
#pragma unroll
    for (int i = 0; i < 4; i++) {
      acc[i][0] = __builtin_amdgcn_mfma_f32_16x16x32_bf16(W10, S1[i], acc[i][0], 0, 0, 0);
      acc[i][1] = __builtin_amdgcn_mfma_f32_16x16x32_bf16(W11, S1[i], acc[i][1], 0, 0, 0);
    }
#pragma unroll
    for (int i = 0; i < 8; i++) {
      __builtin_amdgcn_sched_group_barrier(0x008, 1, 1);
      __builtin_amdgcn_sched_group_barrier(0x100, 1, 1);
    }
    SCHED();
  }
  const int col = ns * 128 + wn * 32 + (g & 1) * 16 + (g >> 1) * 8;
#pragma unroll
  for (int i = 0; i < 4; i++) {
    const int jt = 4 * wm + i;
    const int tl = 128 * half + 16 * (u & 7) + 4 * (2 * (jt & 1) + (u >> 3)) + (jt >> 1);
    unsigned a0 = pack_bf16(acc[i][0][0], acc[i][0][1]), a1 = pack_bf16(acc[i][0][2], acc[i][0][3]);
    unsigned b0 = pack_bf16(acc[i][1][0], acc[i][1][1]), b1 = pack_bf16(acc[i][1][2], acc[i][1][3]);
    u32x2 r0 = __builtin_amdgcn_permlane16_swap(a0, b0, false, false);
    u32x2 r1 = __builtin_amdgcn_permlane16_swap(a1, b1, false, false);
    u32x4 v = {r0[0], r1[0], r0[1], r1[1]};
    if (tl < 250) *(u32x4*)(out + (size_t)(rb * 250 + tl) * 512 + col) = v;
  }
}


#define STAMP(t) asm volatile("s_memtime %0\n\ts_waitcnt lgkmcnt(0)" : "=s"(t)::"memory")
__device__ __forceinline__ void gl(const void* sbase, unsigned voff, unsigned lds) {
  asm volatile("s_mov_b32 m0, %0\n\ts_nop 0\n\tglobal_load_lds_dwordx4 %1, %2 " ::"s"(lds), "v"(voff), "s"(sbase));
}
__global__ __launch_bounds__(512, 2) void k0(const char* __restrict__ x, const char* __restrict__ w, char* ws) {
  extern __shared__ __attribute__((aligned(16))) char sm[];
  unsigned long long t0, t1; STAMP(t0);
  unsigned rt = (unsigned)__builtin_amdgcn_s_memrealtime();
  const int l = threadIdx.x & 63, wv = __builtin_amdgcn_readfirstlane(threadIdx.x >> 6);
  const int b = blockIdx.x, j = b >> 3, rb = (b & 7) * 4 + (j >> 3), cs = j & 7;
  unsigned lds0 = lds_addr(sm);
  const int rot = (0 * cs + 0 * (rb & 3) + 0 * (b & 7)) & 7;
  const char* base = x + (size_t)rb * 250 * 1024;
  unsigned xo = (l >> 3) * 1024 + (l & 7) * 16, wo = (l >> 4) * 2048 + ((l >> 3) & 1) * 1024 + (l & 7) * 16;
  if (0 == 0) {
    const char* p = x + (size_t)(rb * 250 + cs * 31) * 1024;
#pragma unroll
    for (int i = 0; i < 4; i++) { int r = i * 8 + wv; if (r < 31) gl(p + r * 1024, l * 16, lds0 + r * 1024); }
  } else if (0 == 1) {
    if (wv == 0) gl(x + (size_t)(rb * 250 + cs * 31) * 1024, l * 16, lds0);
  } else {
#pragma unroll
    for (int s = 0; s < 0 - 1; s++) {
      int kt = s ^ rot;
      unsigned d = lds0 + (s % 5) * 32768 + wv * 1024;
#pragma unroll
      for (int q = 0; q < 2; q++) gl(w + (kt * 64 + q * 32 + wv * 4) * 2048 + cs * 128, wo, d + q * 8192);
#pragma unroll
      for (int q = 0; q < 2; q++) gl(base + kt * 128 + (q * 8 + wv) * 8192, xo, d + (2 + q) * 8192);
      __builtin_amdgcn_s_barrier();
    }
  }
  asm volatile("s_waitcnt vmcnt(0)" ::: "memory");
  __builtin_amdgcn_s_barrier();
  STAMP(t1);
  if (threadIdx.x == 0) { unsigned* o = (unsigned*)(ws + (64u << 20)); o[b * 2] = (unsigned)(t1 - t0); o[b * 2 + 1] = rt; }
}
__global__ void k3(char* ws) {
  const unsigned* o = (const unsigned*)(ws + (64u << 20));
  __shared__ unsigned sum, mx, smin;
  if (threadIdx.x == 0) { sum = 0; mx = 0; smin = ~0u; }
  __syncthreads();
  atomicAdd(&sum, o[threadIdx.x * 2]); atomicMax(&mx, o[threadIdx.x * 2]); atomicMin(&smin, o[threadIdx.x * 2 + 1]);
  __syncthreads();
  if (threadIdx.x == 0) {
    unsigned mean = (sum >> 8) / 10, max = mx / 10, zero = 0;
    (void)mean; (void)max; (void)zero;
    unsigned v = zero;
    if (v > 20000u) v = 20000u;
    unsigned target = 30000u + v;
    while ((unsigned)__builtin_amdgcn_s_memrealtime() - smin < target) __builtin_amdgcn_s_sleep(1);
  }
}

extern "C" void kernel_launch(void* const* d_in, const int* in_sizes, int n_in, void* d_out, int out_size,
                              void* d_ws, size_t ws_size, hipStream_t stream) {
  const u16* x = (const u16*)d_in[0];
  const u16* w1 = (const u16*)d_in[1];
  const u16* dw = (const u16*)d_in[2];
  const u16* w2 = (const u16*)d_in[3];
  (void)hipFuncSetAttribute((const void*)k1, hipFuncAttributeMaxDynamicSharedMemorySize, K1_LDS);
  (void)hipFuncSetAttribute((const void*)k2, hipFuncAttributeMaxDynamicSharedMemorySize, K2_LDS);
  (void)hipFuncSetAttribute((const void*)k0, hipFuncAttributeMaxDynamicSharedMemorySize, 163840);
  k0<<<256, 512, 163840, stream>>>((const char*)x, (const char*)w1, (char*)d_ws);
  k1<<<256, 512, K1_LDS, stream>>>(x, w1, dw, (char*)d_ws);
  k2<<<256, 512, K2_LDS, stream>>>((const char*)d_ws, w2, (u16*)d_out);
  k3<<<1, 256, 0, stream>>>((char*)d_ws);
}
```

```cpp
#include <hip/hip_runtime.h>
#include <stdint.h>

typedef unsigned short u16;
typedef __attribute__((ext_vector_type(8))) short bf16x8;
typedef __attribute__((ext_vector_type(4))) short s16x4;
typedef __attribute__((ext_vector_type(4))) float f32x4;
typedef __attribute__((ext_vector_type(2))) float f32x2;
typedef __attribute__((ext_vector_type(2))) __bf16 bf16x2;
typedef __attribute__((ext_vector_type(4))) unsigned u32x4;
typedef __attribute__((ext_vector_type(2))) unsigned u32x2;
#define LDSP(T) __attribute__((address_space(3))) T*
#define GLBP(T) __attribute__((address_space(1))) T*

#define WAIT_V(n) asm volatile("s_waitcnt vmcnt(%0)" ::"n"(n) : "memory")
#define WAIT_L(n) asm volatile("s_waitcnt lgkmcnt(%0)" ::"n"(n) : "memory")
#define BAR() asm volatile("s_barrier" ::: "memory")
#define SCHED() __builtin_amdgcn_sched_barrier(0)

__device__ __forceinline__ unsigned pack_bf16(float a, float b) {
  f32x2 v = {a, b};
  return __builtin_bit_cast(unsigned, __builtin_convertvector(v, bf16x2));
}
__device__ __forceinline__ float sigmoid_mul(float a, float b) {
  return a * __builtin_amdgcn_rcpf(1.f + __builtin_amdgcn_exp2f(b * -1.44269504088896f));
}
__device__ __forceinline__ void glds16(const void* sbase, unsigned voff, unsigned lds) {
  asm volatile("s_mov_b32 m0, %0\n\ts_nop 0\n\tglobal_load_lds_dwordx4 %1, %2" ::"s"(lds), "v"(voff), "s"(sbase));
}
__device__ __forceinline__ unsigned lds_addr(const char* p) { return (unsigned)(size_t)(LDSP(const char))p; }
__device__ __forceinline__ bf16x8 lds_b128(const char* p) { return *(const bf16x8*)p; }
__device__ __forceinline__ bf16x8 lds_tr2(const char* p) {
  s16x4 lo = __builtin_amdgcn_ds_read_tr16_b64_v4i16((LDSP(s16x4))p);
  s16x4 hi = __builtin_amdgcn_ds_read_tr16_b64_v4i16((LDSP(s16x4))(p + 1024));
  return __builtin_shufflevector(lo, hi, 0, 1, 2, 3, 4, 5, 6, 7);
}

constexpr int K1_XB = 272 * 128;
constexpr int K1_WB = 64 * 256;
constexpr int K1_ST = K1_XB + K1_WB;
constexpr int K1_TAB = 3 * K1_ST;
constexpr int K1_LDS = K1_TAB + 8192;
constexpr int HT_STRIDE = 560;

template <int NT, int NG>
__device__ __forceinline__ void k1_body(char* sm, const char* x, const char* w1, const unsigned (&xoff)[5],
                                        const unsigned (&woff)[2], int w, int l, int t0) {
  const int g = l >> 4, m = l & 15;
  const int mt0 = (NT == 9) ? 0 : 9;
  auto issue = [&](int kt, int st) {
    const char* xs = x + kt * 128;
    const char* ws = w1 + kt * (64 * 2048);
    unsigned d = lds_addr(sm) + st;
#pragma unroll
    for (int i = 0; i < 2; i++) glds16(ws, woff[i], d + K1_XB + (2 * w + i) * 1024);
#pragma unroll
    for (int i = 0; i < 4; i++) glds16(xs, xoff[i], d + (w + 8 * i) * 1024);
    if (NG == 7) glds16(xs, xoff[4], d + (32 + w - 6) * 1024);
  };
  const int a_off0 = (mt0 * 16 + m) * 128 + ((g ^ (l & 7)) << 4);
  const int a_off1 = a_off0 ^ 64;
  const int q = m >> 2, p = m & 3, wn = w & 3;
  const int f = q | ((g & 1) << 2);
  const int b_offa = K1_XB + (8 * g + q) * 256 + ((wn ^ f) << 5) + p * 8;
  const int b_offb = b_offa ^ 128;

  f32x4 acca[NT], accb[NT];
#pragma unroll
  for (int i = 0; i < NT; i++) acca[i] = f32x4{0, 0, 0, 0}, accb[i] = f32x4{0, 0, 0, 0};
  bf16x8 A0[NT], A1[NT], Ba0, Bb0, Ba1, Bb1;

  WAIT_V(2 * NG);
  BAR();
  Ba0 = lds_tr2(sm + b_offa);
  Bb0 = lds_tr2(sm + b_offb);
#pragma unroll
  for (int i = 0; i < NT; i++) A0[i] = lds_b128(sm + a_off0 + i * 2048);
  int st = 0;
#pragma unroll 1
  for (int it = 0; it < 8; it++) {
    const char* sc = sm + st;
    Ba1 = lds_tr2(sc + b_offa + 8192);
    Bb1 = lds_tr2(sc + b_offb + 8192);
#pragma unroll
    for (int i = 0; i < NT; i++) A1[i] = lds_b128(sc + a_off1 + i * 2048);
#pragma unroll
    for (int i = 0; i < NT; i++) {
      acca[i] = __builtin_amdgcn_mfma_f32_16x16x32_bf16(A0[i], Ba0, acca[i], 0, 0, 0);
      accb[i] = __builtin_amdgcn_mfma_f32_16x16x32_bf16(A0[i], Bb0, accb[i], 0, 0, 0);
    }
#pragma unroll
    for (int i = 0; i < NT + 4; i++) {
      __builtin_amdgcn_sched_group_barrier(0x008, 1, 0);
      __builtin_amdgcn_sched_group_barrier(0x100, 1, 0);
    }
    __builtin_amdgcn_sched_group_barrier(0x008, NT - 4, 0);
    SCHED();
    int stn = (st == 2 * K1_ST) ? 0 : st + K1_ST;
    if (it < 7) {
      WAIT_L(0);
      if (it < 6) WAIT_V(NG);
      else WAIT_V(0);
      BAR();
      if (it < 5) issue(it + 3, st);
    }
    SCHED();
    const char* sn = sm + stn;
    Ba0 = lds_tr2(sn + b_offa);
    Bb0 = lds_tr2(sn + b_offb);
#pragma unroll
    for (int i = 0; i < NT; i++) A0[i] = lds_b128(sn + a_off0 + i * 2048);
#pragma unroll
    for (int i = 0; i < NT; i++) {
      acca[i] = __builtin_amdgcn_mfma_f32_16x16x32_bf16(A1[i], Ba1, acca[i], 0, 0, 0);
      accb[i] = __builtin_amdgcn_mfma_f32_16x16x32_bf16(A1[i], Bb1, accb[i], 0, 0, 0);
    }
#pragma unroll
    for (int i = 0; i < NT + 4; i++) {
      __builtin_amdgcn_sched_group_barrier(0x008, 1, 1);
      __builtin_amdgcn_sched_group_barrier(0x100, 1, 1);
    }
    __builtin_amdgcn_sched_group_barrier(0x008, NT - 4, 1);
    SCHED();
    st = stn;
  }
  char* ht = sm + (wn * 16 + m) * HT_STRIDE + (mt0 * 16 + 4 * g) * 2;
  const int tb = t0 - 7 + mt0 * 16 + 4 * g;
#pragma unroll
  for (int i = 0; i < NT; i++) {
    float h[4];
#pragma unroll
    for (int r = 0; r < 4; r++) {
      float v = sigmoid_mul(acca[i][r], accb[i][r]);
      h[r] = ((unsigned)(tb + 16 * i + r) < 4000u) ? v : 0.f;
    }
    u32x2 pk = {pack_bf16(h[0], h[1]), pack_bf16(h[2], h[3])};
    *(u32x2*)(ht + i * 32) = pk;
  }
}

__global__ __launch_bounds__(512, 2) void k1(const u16* __restrict__ x, const u16* __restrict__ w1,
                                            const u16* __restrict__ dw, char* __restrict__ s) {
  extern __shared__ __attribute__((aligned(16))) char sm[];
  const int tid = threadIdx.x, l = tid & 63, w = __builtin_amdgcn_readfirstlane(tid >> 6);
  const int b = blockIdx.x, j = b >> 3, rb = (b & 7) * 4 + (j >> 3), cs = j & 7;
  const int batch = rb >> 4, t0 = (rb & 15) * 250;

  unsigned tap[8];
  const int idx0 = (w & 3) * 8 - 8 + (w >> 2);
  {
    const unsigned voff = (cs * 64 + l) * 2;
#pragma unroll
    for (int e = 0; e < 8; e++) {
      int i = idx0 + e;
      const u16* p = dw + ((unsigned)i < 15u ? i : 0) * 512;
      asm volatile("global_load_ushort %0, %1, %2" : "=v"(tap[e]) : "v"(voff), "s"(p));
    }
  }
  unsigned xoff[5], woff[2];
#pragma unroll
  for (int i = 0; i < 5; i++) {
    int rowm = 8 * (i < 4 ? (w + 8 * i) : (32 + (w & 1))) + (l >> 3);
    int t = min(max(t0 - 7 + rowm, 0), 3999);
    xoff[i] = (unsigned)((batch * 4000 + t) * 1024 + (((l & 7) ^ (l >> 3)) << 4));
  }
#pragma unroll
  for (int i = 0; i < 2; i++) {
    int row = 8 * w + 4 * i + (l >> 4);
    int f = (l >> 4) | ((w & 1) << 2);
    int slot = ((l & 15) >> 1) ^ f;
    int col = (slot & 3) * 16 + (l & 1) * 8 + cs * 64 + (slot >> 2) * 512;
    woff[i] = (unsigned)((row * 1024 + col) * 2);
  }
  const char* xb = (const char*)x;
  const char* wb = (const char*)w1;
#pragma unroll
  for (int kt = 0; kt < 3; kt++) {
    const char* xs = xb + kt * 128;
    const char* ws = wb + kt * (64 * 2048);
    unsigned d = lds_addr(sm) + kt * K1_ST;
#pragma unroll
    for (int i = 0; i < 2; i++) glds16(ws, woff[i], d + K1_XB + (2 * w + i) * 1024);
#pragma unroll
    for (int i = 0; i < 4; i++) glds16(xs, xoff[i], d + (w + 8 * i) * 1024);
    if (w >= 6) glds16(xs, xoff[4], d + (32 + w - 6) * 1024);
  }
  {
    asm volatile("s_waitcnt vmcnt(18)"
                 : "+v"(tap[0]), "+v"(tap[1]), "+v"(tap[2]), "+v"(tap[3]), "+v"(tap[4]), "+v"(tap[5]), "+v"(tap[6]),
                   "+v"(tap[7]));
#pragma unroll
    for (int e = 0; e < 8; e++) tap[e] = ((unsigned)(idx0 + e) < 15u) ? tap[e] : 0u;
    u32x4 tv = {tap[0] | (tap[1] << 16), tap[2] | (tap[3] << 16), tap[4] | (tap[5] << 16), tap[6] | (tap[7] << 16)};
    *(u32x4*)(sm + K1_TAB + l * 128 + w * 16) = tv;
  }
  if (w < 4) k1_body<9, 6>(sm, xb, wb, xoff, woff, w, l, t0);
  else if (w < 6) k1_body<8, 6>(sm, xb, wb, xoff, woff, w, l, t0);
  else k1_body<8, 7>(sm, xb, wb, xoff, woff, w, l, t0);
  WAIT_L(0);
  BAR();
  const int g = l >> 4, m = l & 15;
  int start = 8 * g - m;
  if (start < -8 || start > 14) start = -8;
  const char* tp = sm + K1_TAB + (8 * w) * 128 + (start & 1) * 64 + (((start + 8) >> 1) << 2);
  const char* hp = sm + (8 * w) * HT_STRIDE + 32 * m + 16 * g;
  f32x4 o[8];
#pragma unroll
  for (int c = 0; c < 8; c++) {
    const unsigned* t32 = (const unsigned*)(tp + c * 128);
    u32x4 av = {t32[0], t32[1], t32[2], t32[3]};
    bf16x8 hv = lds_b128(hp + c * HT_STRIDE);
    o[c] = __builtin_amdgcn_mfma_f32_16x16x32_bf16(__builtin_bit_cast(bf16x8, av), hv, f32x4{0, 0, 0, 0}, 0, 0, 0);
  }
  char* sp = s + (size_t)(((rb * 2 + (m >> 3)) * 64 + cs * 8 + w) * 2048 + g * 128 + (m & 7) * 16);
#pragma unroll
  for (int r = 0; r < 4; r++) {
    u32x4 v;
#pragma unroll
    for (int c = 0; c < 4; c++)
      v[c] = pack_bf16(sigmoid_mul(o[2 * c][r], o[2 * c][r]), sigmoid_mul(o[2 * c + 1][r], o[2 * c + 1][r]));
    *(u32x4*)(sp + r * 512) = v;
  }
}

constexpr int K2_NS = 5;
constexpr int K2_ST = 32768;
constexpr int K2_LDS = K2_NS * K2_ST;

__global__ __launch_bounds__(512, 2) void k2(const char* __restrict__ s, const u16* __restrict__ w2,
                                            u16* __restrict__ out) {
  extern __shared__ __attribute__((aligned(16))) char sm[];
  const int tid = threadIdx.x, l = tid & 63, w = __builtin_amdgcn_readfirstlane(tid >> 6);
  const int b = blockIdx.x, j = b >> 3, rb = (b & 7) * 4 + (j >> 3), half = (j >> 2) & 1, ns = j & 3;
  const char* sa = s + (size_t)(rb * 2 + half) * 131072 + w * 2048;
  const char* wb = (const char*)w2;
  unsigned woff[2];
#pragma unroll
  for (int i = 0; i < 2; i++) {
    int row = 8 * w + 4 * i + (l >> 4);
    int f = (l >> 4) | ((w & 1) << 2);
    int slot = ((l & 15) >> 1) ^ f;
    woff[i] = (unsigned)((row * 512 + ns * 128 + slot * 16 + (l & 1) * 8) * 2);
  }
  const unsigned aoff = l * 16;
  auto issue = [&](int kt) {
    unsigned d = lds_addr(sm) + (kt % K2_NS) * K2_ST;
#pragma unroll
    for (int i = 0; i < 2; i++) glds16(wb + kt * 65536, woff[i], d + 16384 + (2 * w + i) * 1024);
#pragma unroll
    for (int i = 0; i < 2; i++) glds16(sa + kt * 16384 + i * 1024, aoff, d + (2 * w + i) * 1024);
  };
#pragma unroll
  for (int kt = 0; kt < K2_NS; kt++) issue(kt);

  const int g = l >> 4, u = l & 15, q = u >> 2, p = u & 3, wm = w >> 2, wn = w & 3;
  const int f = q | ((g & 1) << 2);
  const int a_off = g * 2048 + wm * 1024 + u * 16;
  const int b_off0 = 16384 + (8 * g + q) * 256 + (((2 * wn) ^ f) << 5) + p * 8;
  const int b_off1 = b_off0 ^ 32;
  f32x4 acc[4][2];
#pragma unroll
  for (int i = 0; i < 4; i++) acc[i][0] = acc[i][1] = f32x4{0, 0, 0, 0};
  bf16x8 S0[4], S1[4], W00, W01, W10, W11;
  WAIT_V(4 * (K2_NS - 1));
  BAR();
  W00 = lds_tr2(sm + b_off0);
  W01 = lds_tr2(sm + b_off1);
#pragma unroll
  for (int i = 0; i < 4; i++) S0[i] = lds_b128(sm + a_off + i * 256);
#pragma unroll
  for (int it = 0; it < 8; it++) {
    const char* sc = sm + (it % K2_NS) * K2_ST;
    W10 = lds_tr2(sc + b_off0 + 8192);
    W11 = lds_tr2(sc + b_off1 + 8192);
#pragma unroll
    for (int i = 0; i < 4; i++) S1[i] = lds_b128(sc + a_off + 8192 + i * 256);
#pragma unroll
    for (int i = 0; i < 4; i++) {
      acc[i][0] = __builtin_amdgcn_mfma_f32_16x16x32_bf16(W00, S0[i], acc[i][0], 0, 0, 0);
      acc[i][1] = __builtin_amdgcn_mfma_f32_16x16x32_bf16(W01, S0[i], acc[i][1], 0, 0, 0);
    }
#pragma unroll
    for (int i = 0; i < 8; i++) {
      __builtin_amdgcn_sched_group_barrier(0x008, 1, 0);
      __builtin_amdgcn_sched_group_barrier(0x100, 1, 0);
    }
    SCHED();
    if (it < 7) {
      WAIT_L(0);
      constexpr int lastis = 7;
      int hi = it + K2_NS - 1 < lastis ? it + K2_NS - 1 : lastis;
      switch (hi - (it + 1)) {
        case 0: WAIT_V(0); break;
        case 1: WAIT_V(4); break;
        case 2: WAIT_V(8); break;
        case 3: WAIT_V(12); break;
        default: WAIT_V(16); break;
      }
      BAR();
      if (it + K2_NS < 8) issue(it + K2_NS);
      SCHED();
      const char* sn = sm + ((it + 1) % K2_NS) * K2_ST;
      W00 = lds_tr2(sn + b_off0);
      W01 = lds_tr2(sn + b_off1);
#pragma unroll
      for (int i = 0; i < 4; i++) S0[i] = lds_b128(sn + a_off + i * 256);
    }
#pragma unroll
    for (int i = 0; i < 4; i++) {
      acc[i][0] = __builtin_amdgcn_mfma_f32_16x16x32_bf16(W10, S1[i], acc[i][0], 0, 0, 0);
      acc[i][1] = __builtin_amdgcn_mfma_f32_16x16x32_bf16(W11, S1[i], acc[i][1], 0, 0, 0);
    }
#pragma unroll
    for (int i = 0; i < 8; i++) {
      __builtin_amdgcn_sched_group_barrier(0x008, 1, 1);
      __builtin_amdgcn_sched_group_barrier(0x100, 1, 1);
    }
    SCHED();
  }
  const int col = ns * 128 + wn * 32 + (g & 1) * 16 + (g >> 1) * 8;
#pragma unroll
  for (int i = 0; i < 4; i++) {
    const int jt = 4 * wm + i;
    const int tl = 128 * half + 16 * (u & 7) + 4 * (2 * (jt & 1) + (u >> 3)) + (jt >> 1);
    unsigned a0 = pack_bf16(acc[i][0][0], acc[i][0][1]), a1 = pack_bf16(acc[i][0][2], acc[i][0][3]);
    unsigned b0 = pack_bf16(acc[i][1][0], acc[i][1][1]), b1 = pack_bf16(acc[i][1][2], acc[i][1][3]);
    u32x2 r0 = __builtin_amdgcn_permlane16_swap(a0, b0, false, false);
    u32x2 r1 = __builtin_amdgcn_permlane16_swap(a1, b1, false, false);
    u32x4 v = {r0[0], r1[0], r0[1], r1[1]};
    if (tl < 250) *(u32x4*)(out + (size_t)(rb * 250 + tl) * 512 + col) = v;
  }
}


#define STAMP(t) asm volatile("s_memtime %0\n\ts_waitcnt lgkmcnt(0)" : "=s"(t)::"memory")
__device__ __forceinline__ void gl(const void* sbase, unsigned voff, unsigned lds) {
  asm volatile("s_mov_b32 m0, %0\n\ts_nop 0\n\tglobal_load_lds_dwordx4 %1, %2 " ::"s"(lds), "v"(voff), "s"(sbase));
}
__global__ __launch_bounds__(512, 2) void k0(const char* __restrict__ x, const char* __restrict__ w, char* ws) {
  extern __shared__ __attribute__((aligned(16))) char sm[];
  unsigned long long t0, t1; STAMP(t0);
  unsigned rt = (unsigned)__builtin_amdgcn_s_memrealtime();
  const int l = threadIdx.x & 63, wv = __builtin_amdgcn_readfirstlane(threadIdx.x >> 6);
  const int b = blockIdx.x, j = b >> 3, rb = (b & 7) * 4 + (j >> 3), cs = j & 7;
  unsigned lds0 = lds_addr(sm);
  const int rot = (0 * cs + 0 * (rb & 3) + 0 * (b & 7)) & 7;
  const char* base = x + (size_t)rb * 250 * 1024;
  unsigned xo = (l >> 3) * 1024 + (l & 7) * 16, wo = (l >> 4) * 2048 + ((l >> 3) & 1) * 1024 + (l & 7) * 16;
  if (2 == 0) {
    const char* p = x + (size_t)(rb * 250 + cs * 31) * 1024;
#pragma unroll
    for (int i = 0; i < 4; i++) { int r = i * 8 + wv; if (r < 31) gl(p + r * 1024, l * 16, lds0 + r * 1024); }
  } else if (2 == 1) {
    if (wv == 0) gl(x + (size_t)(rb * 250 + cs * 31) * 1024, l * 16, lds0);
  } else {
#pragma unroll
    for (int s = 0; s < 2 - 1; s++) {
      int kt = s ^ rot;
      unsigned d = lds0 + (s % 5) * 32768 + wv * 1024;
#pragma unroll
      for (int q = 0; q < 2; q++) gl(w + (kt * 64 + q * 32 + wv * 4) * 2048 + cs * 128, wo, d + q * 8192);
#pragma unroll
      for (int q = 0; q < 2; q++) gl(base + kt * 128 + (q * 8 + wv) * 8192, xo, d + (2 + q) * 8192);
      __builtin_amdgcn_s_barrier();
    }
  }
  asm volatile("s_waitcnt vmcnt(0)" ::: "memory");
  __builtin_amdgcn_s_barrier();
  STAMP(t1);
  if (threadIdx.x == 0) { unsigned* o = (unsigned*)(ws + (64u << 20)); o[b * 2] = (unsigned)(t1 - t0); o[b * 2 + 1] = rt; }
}
__global__ void k3(char* ws) {
  const unsigned* o = (const unsigned*)(ws + (64u << 20));
  __shared__ unsigned sum, mx, smin;
  if (threadIdx.x == 0) { sum = 0; mx = 0; smin = ~0u; }
  __syncthreads();
  atomicAdd(&sum, o[threadIdx.x * 2]); atomicMax(&mx, o[threadIdx.x * 2]); atomicMin(&smin, o[threadIdx.x * 2 + 1]);
  __syncthreads();
  if (threadIdx.x == 0) {
    unsigned mean = (sum >> 8) / 10, max = mx / 10, zero = 0;
    (void)mean; (void)max; (void)zero;
    unsigned v = mean;
    if (v > 20000u) v = 20000u;
    unsigned target = 30000u + v;
    while ((unsigned)__builtin_amdgcn_s_memrealtime() - smin < target) __builtin_amdgcn_s_sleep(1);
  }
}

extern "C" void kernel_launch(void* const* d_in, const int* in_sizes, int n_in, void* d_out, int out_size,
                              void* d_ws, size_t ws_size, hipStream_t stream) {
  const u16* x = (const u16*)d_in[0];
  const u16* w1 = (const u16*)d_in[1];
  const u16* dw = (const u16*)d_in[2];
  const u16* w2 = (const u16*)d_in[3];
  (void)hipFuncSetAttribute((const void*)k1, hipFuncAttributeMaxDynamicSharedMemorySize, K1_LDS);
  (void)hipFuncSetAttribute((const void*)k2, hipFuncAttributeMaxDynamicSharedMemorySize, K2_LDS);
  (void)hipFuncSetAttribute((const void*)k0, hipFuncAttributeMaxDynamicSharedMemorySize, 163840);
  k0<<<256, 512, 163840, stream>>>((const char*)x, (const char*)w1, (char*)d_ws);
  k1<<<256, 512, K1_LDS, stream>>>(x, w1, dw, (char*)d_ws);
  k2<<<256, 512, K2_LDS, stream>>>((const char*)d_ws, w2, (u16*)d_out);
  k3<<<1, 256, 0, stream>>>((char*)d_ws);
}
```

```cpp
#include <hip/hip_runtime.h>
#include <stdint.h>

typedef unsigned short u16;
typedef __attribute__((ext_vector_type(8))) short bf16x8;
typedef __attribute__((ext_vector_type(4))) short s16x4;
typedef __attribute__((ext_vector_type(4))) float f32x4;
typedef __attribute__((ext_vector_type(2))) float f32x2;
typedef __attribute__((ext_vector_type(2))) __bf16 bf16x2;
typedef __attribute__((ext_vector_type(4))) unsigned u32x4;
typedef __attribute__((ext_vector_type(2))) unsigned u32x2;
#define LDSP(T) __attribute__((address_space(3))) T*
#define GLBP(T) __attribute__((address_space(1))) T*

#define WAIT_V(n) asm volatile("s_waitcnt vmcnt(%0)" ::"n"(n) : "memory")
#define WAIT_L(n) asm volatile("s_waitcnt lgkmcnt(%0)" ::"n"(n) : "memory")
#define BAR() asm volatile("s_barrier" ::: "memory")
#define SCHED() __builtin_amdgcn_sched_barrier(0)

__device__ __forceinline__ unsigned pack_bf16(float a, float b) {
  f32x2 v = {a, b};
  return __builtin_bit_cast(unsigned, __builtin_convertvector(v, bf16x2));
}
__device__ __forceinline__ float sigmoid_mul(float a, float b) {
  return a * __builtin_amdgcn_rcpf(1.f + __builtin_amdgcn_exp2f(b * -1.44269504088896f));
}
__device__ __forceinline__ void glds16(const void* sbase, unsigned voff, unsigned lds) {
  asm volatile("s_mov_b32 m0, %0\n\ts_nop 0\n\tglobal_load_lds_dwordx4 %1, %2" ::"s"(lds), "v"(voff), "s"(sbase));
}
__device__ __forceinline__ unsigned lds_addr(const char* p) { return (unsigned)(size_t)(LDSP(const char))p; }
__device__ __forceinline__ bf16x8 lds_b128(const char* p) { return *(const bf16x8*)p; }
__device__ __forceinline__ bf16x8 lds_tr2(const char* p) {
  s16x4 lo = __builtin_amdgcn_ds_read_tr16_b64_v4i16((LDSP(s16x4))p);
  s16x4 hi = __builtin_amdgcn_ds_read_tr16_b64_v4i16((LDSP(s16x4))(p + 1024));
  return __builtin_shufflevector(lo, hi, 0, 1, 2, 3, 4, 5, 6, 7);
}

constexpr int K1_XB = 272 * 128;
constexpr int K1_WB = 64 * 256;
constexpr int K1_ST = K1_XB + K1_WB;
constexpr int K1_TAB = 3 * K1_ST;
constexpr int K1_LDS = K1_TAB + 8192;
constexpr int HT_STRIDE = 560;

template <int NT, int NG>
__device__ __forceinline__ void k1_body(char* sm, const char* x, const char* w1, const unsigned (&xoff)[5],
                                        const unsigned (&woff)[2], int w, int l, int t0) {
  const int g = l >> 4, m = l & 15;
  const int mt0 = (NT == 9) ? 0 : 9;
  auto issue = [&](int kt, int st) {
    const char* xs = x + kt * 128;
    const char* ws = w1 + kt * (64 * 2048);
    unsigned d = lds_addr(sm) + st;
#pragma unroll
    for (int i = 0; i < 2; i++) glds16(ws, woff[i], d + K1_XB + (2 * w + i) * 1024);
#pragma unroll
    for (int i = 0; i < 4; i++) glds16(xs, xoff[i], d + (w + 8 * i) * 1024);
    if (NG == 7) glds16(xs, xoff[4], d + (32 + w - 6) * 1024);
  };
  const int a_off0 = (mt0 * 16 + m) * 128 + ((g ^ (l & 7)) << 4);
  const int a_off1 = a_off0 ^ 64;
  const int q = m >> 2, p = m & 3, wn = w & 3;
  const int f = q | ((g & 1) << 2);
  const int b_offa = K1_XB + (8 * g + q) * 256 + ((wn ^ f) << 5) + p * 8;
  const int b_offb = b_offa ^ 128;

  f32x4 acca[NT], accb[NT];
#pragma unroll
  for (int i = 0; i < NT; i++) acca[i] = f32x4{0, 0, 0, 0}, accb[i] = f32x4{0, 0, 0, 0};
  bf16x8 A0[NT], A1[NT], Ba0, Bb0, Ba1, Bb1;

  WAIT_V(2 * NG);
  BAR();
  Ba0 = lds_tr2(sm + b_offa);
  Bb0 = lds_tr2(sm + b_offb);
#pragma unroll
  for (int i = 0; i < NT; i++) A0[i] = lds_b128(sm + a_off0 + i * 2048);
  int st = 0;
#pragma unroll 1
  for (int it = 0; it < 8; it++) {
    const char* sc = sm + st;
    Ba1 = lds_tr2(sc + b_offa + 8192);
    Bb1 = lds_tr2(sc + b_offb + 8192);
#pragma unroll
    for (int i = 0; i < NT; i++) A1[i] = lds_b128(sc + a_off1 + i * 2048);
#pragma unroll
    for (int i = 0; i < NT; i++) {
      acca[i] = __builtin_amdgcn_mfma_f32_16x16x32_bf16(A0[i], Ba0, acca[i], 0, 0, 0);
      accb[i] = __builtin_amdgcn_mfma_f32_16x16x32_bf16(A0[i], Bb0, accb[i], 0, 0, 0);
    }
#pragma unroll
    for (int i = 0; i < NT + 4; i++) {
      __builtin_amdgcn_sched_group_barrier(0x008, 1, 0);
      __builtin_amdgcn_sched_group_barrier(0x100, 1, 0);
    }
    __builtin_amdgcn_sched_group_barrier(0x008, NT - 4, 0);
    SCHED();
    int stn = (st == 2 * K1_ST) ? 0 : st + K1_ST;
    if (it < 7) {
      WAIT_L(0);
      if (it < 6) WAIT_V(NG);
      else WAIT_V(0);
      BAR();
      if (it < 5) issue(it + 3, st);
    }
    SCHED();
    const char* sn = sm + stn;
    Ba0 = lds_tr2(sn + b_offa);
    Bb0 = lds_tr2(sn + b_offb);
#pragma unroll
    for (int i = 0; i < NT; i++) A0[i] = lds_b128(sn + a_off0 + i * 2048);
#pragma unroll
    for (int i = 0; i < NT; i++) {
      acca[i] = __builtin_amdgcn_mfma_f32_16x16x32_bf16(A1[i], Ba1, acca[i], 0, 0, 0);
      accb[i] = __builtin_amdgcn_mfma_f32_16x16x32_bf16(A1[i], Bb1, accb[i], 0, 0, 0);
    }
#pragma unroll
    for (int i = 0; i < NT + 4; i++) {
      __builtin_amdgcn_sched_group_barrier(0x008, 1, 1);
      __builtin_amdgcn_sched_group_barrier(0x100, 1, 1);
    }
    __builtin_amdgcn_sched_group_barrier(0x008, NT - 4, 1);
    SCHED();
    st = stn;
  }
  char* ht = sm + (wn * 16 + m) * HT_STRIDE + (mt0 * 16 + 4 * g) * 2;
  const int tb = t0 - 7 + mt0 * 16 + 4 * g;
#pragma unroll
  for (int i = 0; i < NT; i++) {
    float h[4];
#pragma unroll
    for (int r = 0; r < 4; r++) {
      float v = sigmoid_mul(acca[i][r], accb[i][r]);
      h[r] = ((unsigned)(tb + 16 * i + r) < 4000u) ? v : 0.f;
    }
    u32x2 pk = {pack_bf16(h[0], h[1]), pack_bf16(h[2], h[3])};
    *(u32x2*)(ht + i * 32) = pk;
  }
}

__global__ __launch_bounds__(512, 2) void k1(const u16* __restrict__ x, const u16* __restrict__ w1,
                                            const u16* __restrict__ dw, char* __restrict__ s, const u16* __restrict__ w2) {
  extern __shared__ __attribute__((aligned(16))) char sm[];
  const int tid = threadIdx.x, l = tid & 63, w = __builtin_amdgcn_readfirstlane(tid >> 6);
  const int b = blockIdx.x, j = b >> 3, rb = (b & 7) * 4 + (j >> 3), cs = j & 7;
  const int batch = rb >> 4, t0 = (rb & 15) * 250;

  unsigned pf0 = 0;
  if (w == 7) {
    unsigned long long pc;
    asm volatile("s_getpc_b64 %0" : "=s"(pc));
    unsigned off = min((unsigned)((j & 1) * 8192 + l * 128), 15360u);
    asm volatile("global_load_dword %0, %1, %2" : "=v"(pf0) : "v"(off), "s"(pc & ~255ull));
  }
  unsigned tap[8];
  const int idx0 = (w & 3) * 8 - 8 + (w >> 2);
  {
    const unsigned voff = (cs * 64 + l) * 2;
#pragma unroll
    for (int e = 0; e < 8; e++) {
      int i = idx0 + e;
      const u16* p = dw + ((unsigned)i < 15u ? i : 0) * 512;
      asm volatile("global_load_ushort %0, %1, %2" : "=v"(tap[e]) : "v"(voff), "s"(p));
    }
  }
  unsigned xoff[5], woff[2];
#pragma unroll
  for (int i = 0; i < 5; i++) {
    int rowm = 8 * (i < 4 ? (w + 8 * i) : (32 + (w & 1))) + (l >> 3);
    int t = min(max(t0 - 7 + rowm, 0), 3999);
    xoff[i] = (unsigned)((batch * 4000 + t) * 1024 + (((l & 7) ^ (l >> 3)) << 4));
  }
#pragma unroll
  for (int i = 0; i < 2; i++) {
    int row = 8 * w + 4 * i + (l >> 4);
    int f = (l >> 4) | ((w & 1) << 2);
    int slot = ((l & 15) >> 1) ^ f;
    int col = (slot & 3) * 16 + (l & 1) * 8 + cs * 64 + (slot >> 2) * 512;
    woff[i] = (unsigned)((row * 1024 + col) * 2);
  }
  const char* xb = (const char*)x;
  const char* wb = (const char*)w1;
#pragma unroll
  for (int kt = 0; kt < 3; kt++) {
    const char* xs = xb + kt * 128;
    const char* ws = wb + kt * (64 * 2048);
    unsigned d = lds_addr(sm) + kt * K1_ST;
#pragma unroll
    for (int i = 0; i < 2; i++) glds16(ws, woff[i], d + K1_XB + (2 * w + i) * 1024);
#pragma unroll
    for (int i = 0; i < 4; i++) glds16(xs, xoff[i], d + (w + 8 * i) * 1024);
    if (w >= 6) glds16(xs, xoff[4], d + (32 + w - 6) * 1024);
  }
  {
    asm volatile("s_waitcnt vmcnt(18)"
                 : "+v"(tap[0]), "+v"(tap[1]), "+v"(tap[2]), "+v"(tap[3]), "+v"(tap[4]), "+v"(tap[5]), "+v"(tap[6]),
                   "+v"(tap[7]), "+v"(pf0));
#pragma unroll
    for (int e = 0; e < 8; e++) tap[e] = ((unsigned)(idx0 + e) < 15u) ? tap[e] : 0u;
    u32x4 tv = {tap[0] | (tap[1] << 16), tap[2] | (tap[3] << 16), tap[4] | (tap[5] << 16), tap[6] | (tap[7] << 16)};
    *(u32x4*)(sm + K1_TAB + l * 128 + w * 16) = tv;
  }
  if (w < 4) k1_body<9, 6>(sm, xb, wb, xoff, woff, w, l, t0);
  else if (w < 6) k1_body<8, 6>(sm, xb, wb, xoff, woff, w, l, t0);
  else k1_body<8, 7>(sm, xb, wb, xoff, woff, w, l, t0);
  if (w >= 6) {
    unsigned off = (unsigned)(j * 16384 + (w & 1) * 8192 + l * 128);
    asm volatile("global_load_dword %0, %1, %2" : "=v"(pf0) : "v"(off), "s"(w2));
  }
  WAIT_L(0);
  BAR();
  const int g = l >> 4, m = l & 15;
  int start = 8 * g - m;
  if (start < -8 || start > 14) start = -8;
  const char* tp = sm + K1_TAB + (8 * w) * 128 + (start & 1) * 64 + (((start + 8) >> 1) << 2);
  const char* hp = sm + (8 * w) * HT_STRIDE + 32 * m + 16 * g;
  f32x4 o[8];
#pragma unroll
  for (int c = 0; c < 8; c++) {
    const unsigned* t32 = (const unsigned*)(tp + c * 128);
    u32x4 av = {t32[0], t32[1], t32[2], t32[3]};
    bf16x8 hv = lds_b128(hp + c * HT_STRIDE);
    o[c] = __builtin_amdgcn_mfma_f32_16x16x32_bf16(__builtin_bit_cast(bf16x8, av), hv, f32x4{0, 0, 0, 0}, 0, 0, 0);
  }
  char* sp = s + (size_t)(((rb * 2 + (m >> 3)) * 64 + cs * 8 + w) * 2048 + g * 128 + (m & 7) * 16);
#pragma unroll
  for (int r = 0; r < 4; r++) {
    u32x4 v;
#pragma unroll
    for (int c = 0; c < 4; c++)
      v[c] = pack_bf16(sigmoid_mul(o[2 * c][r], o[2 * c][r]), sigmoid_mul(o[2 * c + 1][r], o[2 * c + 1][r]));
    *(u32x4*)(sp + r * 512) = v;
  }
  asm volatile("s_waitcnt vmcnt(0)" : "+v"(pf0));
}

constexpr int K2_NS = 5;
constexpr int K2_ST = 32768;
constexpr int K2_LDS = K2_NS * K2_ST;

__global__ __launch_bounds__(512, 2) void k2(const char* __restrict__ s, const u16* __restrict__ w2,
                                            u16* __restrict__ out) {
  extern __shared__ __attribute__((aligned(16))) char sm[];
  const int tid = threadIdx.x, l = tid & 63, w = __builtin_amdgcn_readfirstlane(tid >> 6);
  const int b = blockIdx.x, j = b >> 3, rb = (b & 7) * 4 + (j >> 3), half = (j >> 2) & 1, ns = j & 3;
  const char* sa = s + (size_t)(rb * 2 + half) * 131072 + w * 2048;
  const char* wb = (const char*)w2;
  unsigned woff[2];
#pragma unroll
  for (int i = 0; i < 2; i++) {
    int row = 8 * w + 4 * i + (l >> 4);
    int f = (l >> 4) | ((w & 1) << 2);
    int slot = ((l & 15) >> 1) ^ f;
    woff[i] = (unsigned)((row * 512 + ns * 128 + slot * 16 + (l & 1) * 8) * 2);
  }
  const unsigned aoff = l * 16;
  auto issue = [&](int kt) {
    unsigned d = lds_addr(sm) + (kt % K2_NS) * K2_ST;
#pragma unroll
    for (int i = 0; i < 2; i++) glds16(wb + kt * 65536, woff[i], d + 16384 + (2 * w + i) * 1024);
#pragma unroll
    for (int i = 0; i < 2; i++) glds16(sa + kt * 16384 + i * 1024, aoff, d + (2 * w + i) * 1024);
  };
#pragma unroll
  for (int kt = 0; kt < K2_NS; kt++) issue(kt);

  const int g = l >> 4, u = l & 15, q = u >> 2, p = u & 3, wm = w >> 2, wn = w & 3;
  const int f = q | ((g & 1) << 2);
  const int a_off = g * 2048 + wm * 1024 + u * 16;
  const int b_off0 = 16384 + (8 * g + q) * 256 + (((2 * wn) ^ f) << 5) + p * 8;
  const int b_off1 = b_off0 ^ 32;
  f32x4 acc[4][2];
#pragma unroll
  for (int i = 0; i < 4; i++) acc[i][0] = acc[i][1] = f32x4{0, 0, 0, 0};
  bf16x8 S0[4], S1[4], W00, W01, W10, W11;
  WAIT_V(4 * (K2_NS - 1));
  BAR();
  W00 = lds_tr2(sm + b_off0);
  W01 = lds_tr2(sm + b_off1);
#pragma unroll
  for (int i = 0; i < 4; i++) S0[i] = lds_b128(sm + a_off + i * 256);
#pragma unroll
  for (int it = 0; it < 8; it++) {
    const char* sc = sm + (it % K2_NS) * K2_ST;
    W10 = lds_tr2(sc + b_off0 + 8192);
    W11 = lds_tr2(sc + b_off1 + 8192);
#pragma unroll
    for (int i = 0; i < 4; i++) S1[i] = lds_b128(sc + a_off + 8192 + i * 256);
#pragma unroll
    for (int i = 0; i < 4; i++) {
      acc[i][0] = __builtin_amdgcn_mfma_f32_16x16x32_bf16(W00, S0[i], acc[i][0], 0, 0, 0);
      acc[i][1] = __builtin_amdgcn_mfma_f32_16x16x32_bf16(W01, S0[i], acc[i][1], 0, 0, 0);
    }
#pragma unroll
    for (int i = 0; i < 8; i++) {
      __builtin_amdgcn_sched_group_barrier(0x008, 1, 0);
      __builtin_amdgcn_sched_group_barrier(0x100, 1, 0);
    }
    SCHED();
    if (it < 7) {
      WAIT_L(0);
      constexpr int lastis = 7;
      int hi = it + K2_NS - 1 < lastis ? it + K2_NS - 1 : lastis;
      switch (hi - (it + 1)) {
        case 0: WAIT_V(0); break;
        case 1: WAIT_V(4); break;
        case 2: WAIT_V(8); break;
        case 3: WAIT_V(12); break;
        default: WAIT_V(16); break;
      }
      BAR();
      if (it + K2_NS < 8) issue(it + K2_NS);
      SCHED();
      const char* sn = sm + ((it + 1) % K2_NS) * K2_ST;
      W00 = lds_tr2(sn + b_off0);
      W01 = lds_tr2(sn + b_off1);
#pragma unroll
      for (int i = 0; i < 4; i++) S0[i] = lds_b128(sn + a_off + i * 256);
    }
#pragma unroll
    for (int i = 0; i < 4; i++) {
      acc[i][0] = __builtin_amdgcn_mfma_f32_16x16x32_bf16(W10, S1[i], acc[i][0], 0, 0, 0);
      acc[i][1] = __builtin_amdgcn_mfma_f32_16x16x32_bf16(W11, S1[i], acc[i][1], 0, 0, 0);
    }
#pragma unroll
    for (int i = 0; i < 8; i++) {
      __builtin_amdgcn_sched_group_barrier(0x008, 1, 1);
      __builtin_amdgcn_sched_group_barrier(0x100, 1, 1);
    }
    SCHED();
  }
  const int col = ns * 128 + wn * 32 + (g & 1) * 16 + (g >> 1) * 8;
#pragma unroll
  for (int i = 0; i < 4; i++) {
    const int jt = 4 * wm + i;
    const int tl = 128 * half + 16 * (u & 7) + 4 * (2 * (jt & 1) + (u >> 3)) + (jt >> 1);
    unsigned a0 = pack_bf16(acc[i][0][0], acc[i][0][1]), a1 = pack_bf16(acc[i][0][2], acc[i][0][3]);
    unsigned b0 = pack_bf16(acc[i][1][0], acc[i][1][1]), b1 = pack_bf16(acc[i][1][2], acc[i][1][3]);
    u32x2 r0 = __builtin_amdgcn_permlane16_swap(a0, b0, false, false);
    u32x2 r1 = __builtin_amdgcn_permlane16_swap(a1, b1, false, false);
    u32x4 v = {r0[0], r1[0], r0[1], r1[1]};
    if (tl < 250) *(u32x4*)(out + (size_t)(rb * 250 + tl) * 512 + col) = v;
  }
}

extern "C" void kernel_launch(void* const* d_in, const int* in_sizes, int n_in, void* d_out, int out_size,
                              void* d_ws, size_t ws_size, hipStream_t stream) {
  const u16* x = (const u16*)d_in[0];
  const u16* w1 = (const u16*)d_in[1];
  const u16* dw = (const u16*)d_in[2];
  const u16* w2 = (const u16*)d_in[3];
  (void)hipFuncSetAttribute((const void*)k1, hipFuncAttributeMaxDynamicSharedMemorySize, K1_LDS);
  (void)hipFuncSetAttribute((const void*)k2, hipFuncAttributeMaxDynamicSharedMemorySize, K2_LDS);
  k1<<<256, 512, K1_LDS, stream>>>(x, w1, dw, (char*)d_ws, w2);
  k2<<<256, 512, K2_LDS, stream>>>((const char*)d_ws, w2, (u16*)d_out);
}
```

```cpp
#include <hip/hip_runtime.h>
#include <stdint.h>

typedef unsigned short u16;
typedef unsigned long long u64;
typedef __attribute__((ext_vector_type(4))) short s16x4;
typedef __attribute__((ext_vector_type(4))) float f32x4;
typedef __attribute__((ext_vector_type(2))) float f32x2;
typedef __attribute__((ext_vector_type(2))) __bf16 bf16x2;
typedef __attribute__((ext_vector_type(4))) unsigned u32x4;
typedef __attribute__((ext_vector_type(2))) unsigned u32x2;
#define LP(T, a, imm) ((__attribute__((address_space(3))) T*)((__attribute__((address_space(3))) char*)(a) + (imm)))

constexpr int L_FULL = 0;
constexpr int L_GP = 1024;
constexpr int L_DUMMY = 2048;
constexpr int L_HT = 2304;
constexpr int L_RING = 29952;
constexpr int L_STG = 46336;
constexpr int L_TAB = 161024;
constexpr int HSTR = 432;
constexpr int L_SIZE = 163680;
constexpr int CODE_BYTES = 0;
constexpr int G_PRIO = 1;
constexpr int C_PRIO = 2;
constexpr int C_PRIO2 = 1;
constexpr int G_DELAY = -1;
constexpr int ROT2 = 0;
constexpr int C_LOW = 0;
constexpr int P_MASK = 65535;
constexpr int K_SLEEP = 1;
constexpr int N_SLEEP = 0;
constexpr unsigned FLAG_OFF = 8u << 20;
constexpr unsigned EPOCH_OFF = FLAG_OFF + (1u << 20);

#define WAIT_V(n) asm volatile("s_waitcnt vmcnt(%0)" ::"n"(n) : "memory")
#define STEP() do { asm volatile("" ::: "memory"); __builtin_amdgcn_sched_barrier(0); } while (0)
#define PIN(x) asm volatile("" : "+v"(x))

__device__ __forceinline__ void glds16(const char* sb, unsigned voff, unsigned lds) {
  asm volatile("s_mov_b32 m0, %0\n\ts_nop 0\n\tglobal_load_lds_dwordx4 %1, %2" ::"s"(lds), "v"(voff), "s"(sb) : "memory");
}
__device__ __forceinline__ void glds16h(const char* sb, unsigned voff, unsigned lds) {
  asm volatile("s_mov_b32 m0, %0\n\ts_mov_b32 exec_hi, 0\n\tglobal_load_lds_dwordx4 %1, %2\n\ts_mov_b32 exec_hi, -1"
               ::"s"(lds), "v"(voff), "s"(sb) : "memory");
}
__device__ __forceinline__ void touch(const char* sb, unsigned voff, unsigned lds) {
  asm volatile("s_mov_b32 m0, %0\n\ts_nop 0\n\tglobal_load_lds_dword %1, %2" ::"s"(lds), "v"(voff), "s"(sb) : "memory");
}
#define LD32(a) (*LP(const volatile unsigned, a, 0))
__device__ const unsigned seq_tab[32] = {0, 1, 2, 3, 4, 5, 6, 7, 8, 9, 10, 11, 12, 13, 14, 15,
                                         16, 17, 18, 19, 20, 21, 22, 23, 24, 25, 26, 27, 28, 29, 30, 31};
#define CHKA(f, n, addr)                                                                          \
  asm volatile("v_cmp_gt_u32 vcc, %3, %0\n\ts_cbranch_vccz .Lw%=\n"                                \
               ".Ls%=:\n\ts_sleep %4\n\tds_read_b32 %0, %2\n\ts_addk_i32 %1, -1\n\ts_cmp_lt_i32 %1, 0\n\t"       \
               "s_waitcnt lgkmcnt(0)\n\ts_cbranch_scc1 .Lw%=\n\tv_cmp_gt_u32 vcc, %3, %0\n\t"      \
               "s_cbranch_vccnz .Ls%=\n.Lw%=:"                                                    \
               : "+v"(f), "+s"(fuel) : "v"(addr), "n"(n), "n"(N_SLEEP) : "vcc", "scc", "memory")


__global__ __launch_bounds__(512, 2) void fused(const char* __restrict__ x, const char* __restrict__ w1,
                                               const char* __restrict__ dw, const char* __restrict__ w2,
                                               char* __restrict__ out, char* __restrict__ ws) {
  extern __shared__ __attribute__((aligned(16))) char sm[];
  const unsigned S = (unsigned)(size_t)(__attribute__((address_space(3))) char*)sm;
  const int tid = threadIdx.x, l = tid & 63, w = __builtin_amdgcn_readfirstlane(tid >> 6);
  const int b = blockIdx.x, rb = (b & 7) * 4 + (b >> 6), cs = (b >> 3) & 7;
  const int g = l >> 4, m = l & 15;
  const int r0 = rb * 250 - 7;
  int fuel = 1 << 22;
  if (__builtin_expect(w < 4, 1)) {
    const int hw = w, rot = (cs + b + ROT2 * (b >> 6)) & 7;
    const unsigned sg = S + L_FULL + hw * 256 + l * 4, fa = S + L_GP + g * 256 + l * 4;
    const int lam = (m >> 1) ^ g;
    const unsigned wo = g * 2048 + (cs * 64 + (lam & 3) * 16 + (lam >> 2) * 512 + (l & 1) * 8) * 2;
    unsigned xo[4];
#pragma unroll
    for (int i = 0; i < 3; i++)
      xo[i] = max(r0 + 8 * (hw + 4 * i) + (l >> 3), 0) * 1024 + (((l & 7) ^ (g + 4 * (hw & 1))) << 4);
    xo[3] = (r0 + 96 + 4 * hw + ((l >> 3) & 3)) * 1024 + (((l & 7) ^ ((2 * hw + g) & 7)) << 4);
    const unsigned vo = g * 1024 + ((cs >> 1) * 128 + ((m >> 1) ^ (2 * g)) * 16 + (l & 1) * 8) * 2;
    const unsigned sw0 = ((l & 7) ^ g) << 4, to0 = (r0 + 112 + (l >> 3)) * 1024 + sw0, to1 = to0 ^ 64;
    const unsigned l16 = l * 16;
    unsigned pf = 0;
    u64 flag, qv;
    unsigned vz = 0;
    PIN(vz);
#define SIG(n) touch((const char*)(seq_tab + (n)), vz, S + L_FULL + hw * 256)
#define HBAR(n) asm volatile("s_waitcnt vmcnt(%0) lgkmcnt(0)\n\ts_barrier" ::"n"(n) : "memory")
#define GS(c, q, i, d) asm volatile("s_mov_b32 m0, %0\n\ts_nop 0\n\tglobal_load_lds_dwordx4 %1, %2 offset:%3 sc1" ::"s"(S + (d) + hw * 4096), \
    "v"(l16), "s"(ws + (rb * 8 + 2 * (c) + (cs & 1)) * 32768 + (q) * 16384 + hw * 4096), "n"((i) * 1024) : "memory")
#define NEED(n) CHKA(pf, n, fa)
#define PRE() pf = LD32(fa)
#define GW(s, i, d) glds16(w1 + (((s) ^ rot) * 64 + 4 * (hw + 4 * (i))) * 2048, wo, S + (d) + (hw + 4 * (i)) * 1024)
#define GV(j, i, d) glds16(w2 + ((j) * 64 + 4 * (hw + 4 * (i))) * 1024, vo, S + (d) + (hw + 4 * (i)) * 1024)
#define GX(s, i, d) do { if ((i) < 3) glds16(x + ((s) ^ rot) * 128, xo[i], S + (d) + (hw + 4 * (i)) * 1024); \
    else glds16h(x + ((s) ^ rot) * 128, xo[3], S + (d) + 12288 + hw * 512); } while (0)
#define GT(t, q, i, d) do { const char* sb_ = x + ((4 * (q) + hw) ^ rot) * 128; \
    if ((t) < 4 || (i) < 2) glds16(sb_ + (32 * (t) + 8 * (i)) * 1024, ((i) & 1) ? to1 : to0, S + (d) + hw * 4096 + (i) * 1024); \
    else glds16(sb_, min(r0 + 256 + (l >> 3), 7999) * 1024 + sw0, S + (d) + hw * 4096 + (i) * 1024); } while (0)
    const unsigned tb = S + L_TAB + hw * 640;
    s16x4 TA[5];
#define LDDW() do { \
    asm volatile("s_mov_b32 m0, %0\n\ts_mov_b64 exec, 0x3fffffff\n\tglobal_load_lds_dwordx4 %1, %2\n\ts_mov_b64 exec, -1" \
                 ::"s"(tb + 96), "v"(min(l >> 1, 14) * 1024 + (cs * 64 + hw * 16 + (l & 1) * 8) * 2), "s"(dw) : "memory"); } while (0)
#define LDEP() do { \
    asm volatile("global_load_dwordx2 %0, %1, %2 sc1" : "=v"(flag) : "v"(0u), "s"(ws + EPOCH_OFF + rb * 128) : "memory"); \
    touch((const char*)seq_tab, vz, S + L_DUMMY); } while (0)
#define ZERO() do { *LP(volatile unsigned, sg, 0) = 0; \
    *LP(u32x4, tb + (m < 6 ? m * 16 : 480 + m * 16), 0) = u32x4{0, 0, 0, 0}; STEP(); } while (0)
#define FLG(n) do { asm volatile("s_waitcnt vmcnt(%1)" : "+v"(flag) : "n"(n) : "memory"); \
    flag += 0x9E3779B97F4A7C15ull; PIN(flag); STEP(); } while (0)
#define TOEP(n) do { WAIT_V(n); \
    const unsigned a_ = tb + (3 - (l & 3)) * 32 + (l >> 2) * 2; \
    _Pragma("unroll") for (int k = 0; k < 5; k++) { \
      TA[k] = s16x4{*LP(short, a_, k * 128), *LP(short, a_, k * 128 + 32), *LP(short, a_, k * 128 + 64), *LP(short, a_, k * 128 + 96)}; \
      } asm volatile("" : "+v"(TA[0]), "+v"(TA[1]), "+v"(TA[2]), "+v"(TA[3]), "+v"(TA[4])); STEP(); } while (0)
    const unsigned hb = S + L_HT + (hw * 16 + (l >> 2)) * HSTR + (l & 3) * 16;
    const unsigned hb6[3] = {hb - ((l & 3) >= 2 ? 416 : 0), hb - ((l & 3) >= 1 ? 416 : 0), hb - 416};
    unsigned sv[8][4];
#define HA(t, k) ((t) < 6 ? hb : hb6[(t) == 6 ? (k) : 2])
#define CCL "memory", "v220", "v221", "v222", "v223", "v224", "v225", "v226", "v227", "v228", "v229", "v230", "v231", "v232", "v233", "v234", "v235", "v236", "v237", "v238", "v239", "v240", "v241", "v242", "v243", "v244", "v245", "v246", "v247"
#define CV0(t) asm volatile("ds_read_b128 v[220:223], %0 offset:%3\nds_read_b128 v[224:227], %1 offset:%4\n" \
    "ds_read_b128 v[228:231], %2 offset:%5" ::"v"(HA(t, 0)), "v"(HA(t, 1)), "v"(HA(t, 2)), "n"(64 * (t)), "n"(64 * (t) + 16), \
    "n"(64 * (t) + 32) : CCL)
#define CV1(t) asm volatile( \
    "s_setprio %[p1]\n" \
    "s_waitcnt lgkmcnt(0)\n" \
    "v_mfma_f32_4x4x4_16b_bf16 v[232:235], %[t0], v[220:221], 0\n" \
    "v_mfma_f32_4x4x4_16b_bf16 v[236:239], %[t0], v[222:223], 0\n" \
    "v_mov_b32 v240, v240\n" \
    "v_mfma_f32_4x4x4_16b_bf16 v[232:235], %[t1], v[222:223], v[232:235]\n" \
    "v_mfma_f32_4x4x4_16b_bf16 v[236:239], %[t1], v[224:225], v[236:239]\n" \
    "v_mov_b32 v240, v240\n" \
    "v_mfma_f32_4x4x4_16b_bf16 v[232:235], %[t2], v[224:225], v[232:235]\n" \
    "v_mfma_f32_4x4x4_16b_bf16 v[236:239], %[t2], v[226:227], v[236:239]\n" \
    "v_mov_b32 v240, v240\n" \
    "v_mfma_f32_4x4x4_16b_bf16 v[232:235], %[t3], v[226:227], v[232:235]\n" \
    "v_mfma_f32_4x4x4_16b_bf16 v[236:239], %[t3], v[228:229], v[236:239]\n" \
    "v_mov_b32 v240, v240\n" \
    "v_mfma_f32_4x4x4_16b_bf16 v[232:235], %[t4], v[228:229], v[232:235]\n" \
    "v_mfma_f32_4x4x4_16b_bf16 v[236:239], %[t4], v[230:231], v[236:239]\n" \
    "s_setprio %[p2]\n" \
    "s_nop 3\n" \
    :: [t0] "v"(TA[0]), [t1] "v"(TA[1]), [t2] "v"(TA[2]), [t3] "v"(TA[3]), [t4] "v"(TA[4]), \
      [p1] "n"(C_PRIO), [p2] "n"((C_LOW >> (t)) & 1 ? 0 : C_PRIO2) : CCL)
#define CV2(t) asm volatile("v_mul_f32 v240, 0xbfb8aa3b, v232\nv_mul_f32 v241, 0xbfb8aa3b, v233\nv_mul_f32 v242, 0xbfb8aa3b, v234\nv_mul_f32 v243, 0xbfb8aa3b, v235\nv_mul_f32 v244, 0xbfb8aa3b, v236\nv_mul_f32 v245, 0xbfb8aa3b, v237\nv_mul_f32 v246, 0xbfb8aa3b, v238\nv_mul_f32 v247, 0xbfb8aa3b, v239" ::: CCL)
#define CV3(t) asm volatile("v_exp_f32 v240, v240\nv_exp_f32 v241, v241\nv_exp_f32 v242, v242\nv_exp_f32 v243, v243" ::: CCL)
#define CV4(t) asm volatile("v_exp_f32 v244, v244\nv_exp_f32 v245, v245\nv_exp_f32 v246, v246\nv_exp_f32 v247, v247" ::: CCL)
#define CV5(t) asm volatile("v_add_f32 v240, 1.0, v240\nv_add_f32 v241, 1.0, v241\nv_add_f32 v242, 1.0, v242\nv_add_f32 v243, 1.0, v243\nv_add_f32 v244, 1.0, v244\nv_add_f32 v245, 1.0, v245\nv_add_f32 v246, 1.0, v246\nv_add_f32 v247, 1.0, v247" ::: CCL)
#define CV6(t) asm volatile("v_rcp_f32 v240, v240\nv_rcp_f32 v241, v241\nv_rcp_f32 v242, v242\nv_rcp_f32 v243, v243" ::: CCL)
#define CV7(t) asm volatile("v_rcp_f32 v244, v244\nv_rcp_f32 v245, v245\nv_rcp_f32 v246, v246\nv_rcp_f32 v247, v247" ::: CCL)
#define CV8(t) asm volatile("v_mul_f32 v232, v232, v240\nv_mul_f32 v233, v233, v241\nv_mul_f32 v234, v234, v242\nv_mul_f32 v235, v235, v243\nv_mul_f32 v236, v236, v244\nv_mul_f32 v237, v237, v245\nv_mul_f32 v238, v238, v246\nv_mul_f32 v239, v239, v247" ::: CCL)
#define CV9(t) asm volatile("v_cvt_pk_bf16_f32 %0, v232, v233\nv_cvt_pk_bf16_f32 %1, v234, v235\nv_cvt_pk_bf16_f32 %2, v236, v237\nv_cvt_pk_bf16_f32 %3, v238, v239\ns_setprio 0" \
    : "=v"(sv[t][0]), "=v"(sv[t][1]), "=v"(sv[t][2]), "=v"(sv[t][3]) :: CCL)
#define SSTORE(t) asm volatile("global_store_dwordx4 %0, %1, %2 sc1\n\ts_nop 1" ::"v"(l16), "v"(u32x4{sv[t][0], sv[t][1], sv[t][2], sv[t][3]}), \
    "s"(ws + (rb * 8 + (t)) * 32768 + cs * 4096 + hw * 1024) : "memory")
#define PUBL(t) asm volatile("s_mov_b64 exec, %3\n\tglobal_store_dwordx2 %0, %1, %2 sc1\n\ts_mov_b64 exec, -1" ::"v"(l * 8), "v"(flag), \
    "s"(ws + FLAG_OFF + ((rb * 8 + (t)) * 8 + cs) * 512 + hw * 128), "n"(P_MASK) : "memory")
#define STOUCH(t) asm volatile("s_mov_b32 m0, %0\n\ts_mov_b64 exec, 255\n\tglobal_load_lds_dword %1, %2 sc1\n\ts_mov_b64 exec, -1" \
    ::"s"(S + L_DUMMY), "v"(l * 128), "s"(ws + (rb * 8 + (t)) * 32768 + cs * 4096 + hw * 1024) : "memory")
#define PREFV() touch(w2, ((b >> 3) * 128 + 32 * hw + (l & 31)) * 128, S + L_DUMMY)
    const unsigned qo = (l & 4) * 512 + (l & 3) * 128;
#define QB(c) (ws + FLAG_OFF + ((rb * 8 + 2 * (c) + (cs & 1)) * 8 + hw) * 512)
#define QLD(c) asm volatile("global_load_dwordx2 %0, %1, %2 sc1" : "=v"(qv) : "v"(qo), "s"(QB(c)) : "memory")
#define KCHK(c, n) \
    asm volatile("s_waitcnt vmcnt(%5)\n\tv_cmp_ne_u64 vcc, %0, %4\n\ts_cbranch_vccz .Lw%=\n" \
                 ".Ls%=:\n\ts_sleep %6\n\tglobal_load_dwordx2 %0, %2, %3 sc1\n\ts_addk_i32 %1, -16\n\ts_cmp_lt_i32 %1, 0\n\t" \
                 "s_waitcnt vmcnt(0)\n\ts_cbranch_scc1 .Lw%=\n\tv_cmp_ne_u64 vcc, %0, %4\n\ts_cbranch_vccnz .Ls%=\n.Lw%=:" \
                 : "+v"(qv), "+s"(fuel) : "v"(qo), "s"(QB(c)), "v"(flag), "n"(n), "n"(K_SLEEP) : "vcc", "scc", "memory")
#define PREF1() touch(x, (r0 + 112 + 16 * hw + m) * 1024 + cs * 128, S + L_DUMMY)
#define PREF2() do { touch(x, min(r0 + 176 + 22 * hw + min(l, 21), 7999) * 1024 + cs * 128, S + L_DUMMY); \
    touch(w2, ((b >> 3) * 128 + 32 * hw + (l & 31)) * 128, S + L_DUMMY); } while (0)
#define HEND(n) do { if (hw == 0 && cs == 0) { \
      const unsigned a_ = S + L_FULL + g * 256 + l * 4; unsigned f_ = 0; CHKA(f_, n, a_); \
      asm volatile("s_mov_b64 exec, 1\n\tglobal_store_dwordx2 %0, %1, %2 sc1\n\ts_mov_b64 exec, -1" ::"v"(l16), "v"(flag), \
                   "s"(ws + EPOCH_OFF + rb * 128) : "memory"); \
    } } while (0)

    ZERO();
    LDDW();
    GW(0, 0, 29952);
    GW(0, 1, 29952);
    GW(0, 2, 29952);
    GW(0, 3, 29952);
    GX(0, 0, 46336);
    GX(0, 1, 46336);
    GX(0, 2, 46336);
    GX(0, 3, 46336);
    GW(1, 0, 62720);
    GW(1, 1, 62720);
    GW(1, 2, 62720);
    GW(1, 3, 62720);
    GX(1, 0, 79104);
    GX(1, 1, 79104);
    GX(1, 2, 79104);
    GX(1, 3, 79104);
    LDEP();
    GW(2, 0, 95488);
    GW(2, 1, 95488);
    GW(2, 2, 95488);
    GW(2, 3, 95488);
    TOEP(22);
    HBAR(14);
    GX(2, 0, 111872);
    GX(2, 1, 111872);
    GX(2, 2, 111872);
    GX(2, 3, 111872);
    GW(3, 0, 128256);
    GW(3, 1, 128256);
    GW(3, 2, 128256);
    GW(3, 3, 128256);
    HBAR(14);
    GX(3, 0, 144640);
    GX(3, 1, 144640);
    GX(3, 2, 144640);
    GX(3, 3, 144640);
    GW(4, 0, 29952);
    GW(4, 1, 29952);
    GW(4, 2, 29952);
    GW(4, 3, 29952);
    HBAR(12);
    GX(4, 0, 46336);
    GX(4, 1, 46336);
    GX(4, 2, 46336);
    GX(4, 3, 46336);
    GW(5, 0, 62720);
    GW(5, 1, 62720);
    GW(5, 2, 62720);
    GW(5, 3, 62720);
    HBAR(12);
    GX(5, 0, 79104);
    GX(5, 1, 79104);
    GX(5, 2, 79104);
    GX(5, 3, 79104);
    GW(6, 0, 95488);
    GW(6, 1, 95488);
    GW(6, 2, 95488);
    GW(6, 3, 95488);
    HBAR(12);
    GX(6, 0, 111872);
    GX(6, 1, 111872);
    GX(6, 2, 111872);
    GX(6, 3, 111872);
    GW(7, 0, 128256);
    GW(7, 1, 128256);
    GW(7, 2, 128256);
    GW(7, 3, 128256);
    HBAR(12);
    GX(7, 0, 144640);
    GX(7, 1, 144640);
    GX(7, 2, 144640);
    GX(7, 3, 144640);
    GT(0, 0, 0, 29952);
    GT(0, 0, 1, 29952);
    GT(0, 0, 2, 29952);
    GT(0, 0, 3, 29952);
    HBAR(12);
    GT(0, 1, 0, 46336);
    GT(0, 1, 1, 46336);
    GT(0, 1, 2, 46336);
    GT(0, 1, 3, 46336);
    SIG(1);
    GT(1, 0, 0, 62720);
    GT(1, 0, 1, 62720);
    GT(1, 0, 2, 62720);
    GT(1, 0, 3, 62720);
    SIG(2);
    HBAR(14);
    GT(1, 1, 0, 79104);
    GT(1, 1, 1, 79104);
    GT(1, 1, 2, 79104);
    GT(1, 1, 3, 79104);
    SIG(3);
    GT(2, 0, 0, 95488);
    GT(2, 0, 1, 95488);
    GT(2, 0, 2, 95488);
    GT(2, 0, 3, 95488);
    SIG(4);
    HBAR(20);
    GT(2, 1, 0, 111872);
    GT(2, 1, 1, 111872);
    GT(2, 1, 2, 111872);
    GT(2, 1, 3, 111872);
    SIG(5);
    GT(3, 0, 0, 128256);
    GT(3, 0, 1, 128256);
    GT(3, 0, 2, 128256);
    GT(3, 0, 3, 128256);
    SIG(6);
    FLG(63);
    PREFV();
    NEED(2);
    GT(3, 1, 0, 144640);
    GT(3, 1, 1, 144640);
    GT(3, 1, 2, 144640);
    GT(3, 1, 3, 144640);
    GT(4, 0, 0, 29952);
    GT(4, 0, 1, 29952);
    GT(4, 0, 2, 29952);
    SIG(7);
    CV0(0);
    GT(4, 1, 0, 46336);
    GT(4, 1, 1, 46336);
    CV1(0);
    GT(4, 1, 2, 46336);
    SIG(8);
    CV2(0);
    CV3(0);
    CV4(0);
    CV5(0);
    CV6(0);
    CV7(0);
    CV8(0);
    CV9(0);
    SSTORE(0);
    NEED(3);
    CV0(1);
    GV(0, 0, 62720);
    GV(0, 1, 62720);
    CV1(1);
    GV(0, 2, 62720);
    CV2(1);
    GV(0, 3, 62720);
    SIG(9);
    CV3(1);
    WAIT_V(5);
    PUBL(0);
    CV4(1);
    CV5(1);
    CV6(1);
    CV7(1);
    CV8(1);
    CV9(1);
    SSTORE(1);
    NEED(4);
    CV0(2);
    GV(1, 0, 79104);
    GV(1, 1, 79104);
    CV1(2);
    GV(1, 2, 79104);
    CV2(2);
    GV(1, 3, 79104);
    SIG(10);
    CV3(2);
    WAIT_V(5);
    PUBL(1);
    CV4(2);
    CV5(2);
    CV6(2);
    CV7(2);
    CV8(2);
    CV9(2);
    SSTORE(2);
    NEED(5);
    CV0(3);
    GV(2, 0, 95488);
    GV(2, 1, 95488);
    CV1(3);
    GV(2, 2, 95488);
    CV2(3);
    GV(2, 3, 95488);
    SIG(11);
    CV3(3);
    WAIT_V(5);
    PUBL(2);
    CV4(3);
    CV5(3);
    CV6(3);
    CV7(3);
    CV8(3);
    CV9(3);
    SSTORE(3);
    NEED(6);
    GV(3, 0, 111872);
    GV(3, 1, 111872);
    GV(3, 2, 111872);
    GV(3, 3, 111872);
    GV(4, 0, 128256);
    GV(4, 1, 128256);
    GV(4, 2, 128256);
    GV(4, 3, 128256);
    SIG(12);
    CV0(4);
    QLD(0);
    WAIT_V(10);
    PUBL(3);
    CV1(4);
    CV2(4);
    CV3(4);
    CV4(4);
    CV5(4);
    CV6(4);
    CV7(4);
    CV8(4);
    CV9(4);
    SSTORE(4);
    KCHK(0, 2);
    NEED(7);
    GS(0, 0, 0, 144640);
    GS(0, 0, 1, 144640);
    GS(0, 0, 2, 144640);
    GS(0, 0, 3, 144640);
    SIG(13);
    CV0(5);
    GV(5, 0, 62720);
    GV(5, 1, 62720);
    CV1(5);
    GV(5, 2, 62720);
    CV2(5);
    GV(5, 3, 62720);
    CV3(5);
    GS(0, 1, 0, 29952);
    CV4(5);
    GS(0, 1, 1, 29952);
    CV5(5);
    GS(0, 1, 2, 29952);
    CV6(5);
    GS(0, 1, 3, 29952);
    SIG(14);
    CV7(5);
    WAIT_V(14);
    PUBL(4);
    CV8(5);
    QLD(1);
    CV9(5);
    SSTORE(5);
    GV(6, 0, 79104);
    GV(6, 1, 79104);
    GV(6, 2, 79104);
    GV(6, 3, 79104);
    SIG(15);
    NEED(8);
    CV0(6);
    GV(7, 0, 46336);
    GV(7, 1, 46336);
    CV1(6);
    GV(7, 2, 46336);
    CV2(6);
    GV(7, 3, 46336);
    SIG(16);
    CV3(6);
    WAIT_V(10);
    PUBL(5);
    CV4(6);
    KCHK(1, 63);
    CV5(6);
    GS(1, 0, 0, 95488);
    CV6(6);
    GS(1, 0, 1, 95488);
    CV7(6);
    GS(1, 0, 2, 95488);
    CV8(6);
    GS(1, 0, 3, 95488);
    SIG(17);
    CV9(6);
    SSTORE(6);
    NEED(9);
    CV0(7);
    CV1(7);
    CV2(7);
    CV3(7);
    CV4(7);
    CV5(7);
    CV6(7);
    CV7(7);
    CV8(7);
    QLD(2);
    CV9(7);
    SSTORE(7);
    GS(1, 1, 0, 111872);
    GS(1, 1, 1, 111872);
    GS(1, 1, 2, 111872);
    GS(1, 1, 3, 111872);
    SIG(18);
    WAIT_V(7);
    PUBL(6);
    WAIT_V(6);
    PUBL(7);
    KCHK(2, 63);
    GS(2, 0, 0, 128256);
    GS(2, 0, 1, 128256);
    GS(2, 0, 2, 128256);
    GS(2, 0, 3, 128256);
    SIG(19);
    NEED(10);
    GS(2, 1, 0, 62720);
    GS(2, 1, 1, 62720);
    GS(2, 1, 2, 62720);
    GS(2, 1, 3, 62720);
    SIG(20);
    __builtin_amdgcn_s_sleep(12);
    QLD(3);
    KCHK(3, 0);
    GS(3, 0, 0, 79104);
    GS(3, 0, 1, 79104);
    GS(3, 0, 2, 79104);
    GS(3, 0, 3, 79104);
    SIG(21);
    GS(3, 1, 0, 144640);
    GS(3, 1, 1, 144640);
    GS(3, 1, 2, 144640);
    GS(3, 1, 3, 144640);
    SIG(22);
    HEND(14);
    WAIT_V(0);
    __builtin_amdgcn_endpgm();
  } else {
    const int gw = w - 4, hh = l >> 5, t = l & 31;
    const unsigned sg = S + L_GP + gw * 256 + l * 4, fa = S + L_FULL + g * 256 + l * 4;
    if (CODE_BYTES) {
      u64 pc;
      asm volatile("s_getpc_b64 %0" : "=s"(pc));
      touch((const char*)(pc & ~255ull), min((cs * 64 + gw * 16 + m) * 128, CODE_BYTES - 128), S + L_DUMMY);
    }
    *LP(volatile unsigned, sg, 0) = 0;
    if (G_DELAY >= 0) {
      const int hw = gw;
      if (G_DELAY) __builtin_amdgcn_s_sleep(G_DELAY & 127);
      PREF1();
      PREF2();
    }
    const int row = (l & 19) | ((l & 4) << 1) | ((l & 8) >> 1);
    const unsigned xa = S + L_RING + row * 128 + ((hh ^ ((row >> 1) & 7)) << 4);
    const unsigned wa = S + L_RING + (8 * hh + (m >> 2)) * 256 + ((((g & 1) * 4 + gw) ^ (m >> 2)) << 5) + (m & 3) * 8;
    const unsigned ha = S + L_HT + (16 * gw + m) * HSTR + (g & 1) * 32 + (g >> 1) * 16;
    const unsigned hq = S + L_HT + (16 * gw + m) * HSTR + g * 8;
    const unsigned wv = S + L_RING + (8 * hh + (m >> 2)) * 256 + (((2 * gw + (g & 1)) ^ (2 * (m >> 2))) << 5) + (m & 1) * 16 + (m & 2) * 4;
    const unsigned sa = S + L_RING + (8 * hh + (m >> 2)) * 64 + (g & 1) * 32 + (m & 3) * 8;
    const unsigned wr = S + L_STG + t * 256 + ((gw ^ (t & 3)) << 6) + ((hh ^ ((t >> 1) & 3)) << 4);
    const unsigned rd = S + L_STG + (l >> 2) * 256 + ((gw ^ ((l >> 2) & 3)) << 6) + (l & 3) * 16;
    const unsigned oo = (l >> 2) * 1024 + (((l & 3) ^ ((l >> 3) & 3)) << 4) + ((cs >> 1) * 128 + 32 * gw) * 2;
    const u64 mf = (rb & 15) == 0 ? 0xffffull : 0ull;
    const u64 ml = 0xffff000000000000ull | ((rb & 15) == 15 ? 0xffff0000ull : 0ull);
    const u64 mo = (cs & 1) ? (1ull << 40) - 1 : ~0ull;
    asm volatile("s_waitcnt lgkmcnt(0)" ::: "memory");
    __builtin_amdgcn_s_setprio(G_PRIO);
    asm volatile(
      "v_xor_b32 v16, 32, %[xa]\n"
      "v_xor_b32 v17, 64, %[xa]\n"
      "v_xor_b32 v18, 0x60, %[xa]\n"
      "v_add_u32 v19, 0x10000, %[xa]\n"
      "v_add_u32 v20, 0x10000, v16\n"
      "v_add_u32 v21, 0x10000, v17\n"
      "v_add_u32 v22, 0x10000, v18\n"
      "v_add_u32 v23, 0x10000, %[wa]\n"
      "v_add_u32 v24, 0x10000, %[wv]\n"
      "v_add_u32 v25, 0x10000, %[sa]\n"
      "v_xor_b32 v26, 32, %[wr]\n"
      "v_mov_b32 v27, 1\n"
      "v_mov_b32 v28, 0\n"
      "s_mov_b32 s90, 0xbfb8aa3b\n"
      "s_mov_b64 s[92:93], %[ob]\n"
      "s_barrier\n"
      "ds_read_b64_tr_b16 v[128:129], %[wa] offset:0\n"
      "ds_read_b64_tr_b16 v[130:131], %[wa] offset:1024\n"
      "ds_read_b128 v[36:39], %[xa] offset:20480\n"
      "ds_read_b128 v[32:35], %[xa] offset:16384\n"
      "ds_read_b128 v[44:47], %[xa] offset:26624\n"
      "ds_read_b128 v[40:43], %[xa] offset:22528\n"
      "ds_read_b64_tr_b16 v[132:133], %[wa] offset:4096\n"
      "ds_read_b64_tr_b16 v[134:135], %[wa] offset:5120\n"
      "ds_read_b128 v[52:55], v16 offset:20480\n"
      "ds_read_b128 v[48:51], v16 offset:16384\n"
      "s_waitcnt lgkmcnt(6)\n"
      "v_mfma_f32_32x32x16_bf16 v[56:71], v[32:35], v[128:131], 0\n"
      "v_mfma_f32_32x32x16_bf16 v[104:119], v[36:39], v[128:131], 0\n"
      "ds_read_b128 v[36:39], v16 offset:26624\n"
      "ds_read_b128 v[32:35], v16 offset:22528\n"
      "s_waitcnt lgkmcnt(6)\n"
      "v_mfma_f32_32x32x16_bf16 v[72:87], v[40:43], v[128:131], 0\n"
      "ds_read_b64_tr_b16 v[136:137], %[wa] offset:8192\n"
      "ds_read_b64_tr_b16 v[138:139], %[wa] offset:9216\n"
      "v_mfma_f32_32x32x16_bf16 v[88:103], v[44:47], v[128:131], 0\n"
      "ds_read_b128 v[44:47], v17 offset:20480\n"
      "ds_read_b128 v[40:43], v17 offset:16384\n"
      "s_waitcnt lgkmcnt(6)\n"
      "v_mfma_f32_32x32x16_bf16 v[56:71], v[48:51], v[132:135], v[56:71]\n"
      "ds_read_b64_tr_b16 v[140:141], %[wa] offset:12288\n"
      "ds_read_b64_tr_b16 v[142:143], %[wa] offset:13312\n"
      "v_mfma_f32_32x32x16_bf16 v[104:119], v[52:55], v[132:135], v[104:119]\n"
      "ds_read_b128 v[52:55], v17 offset:26624\n"
      "ds_read_b128 v[48:51], v17 offset:22528\n"
      "s_waitcnt lgkmcnt(8)\n"
      "v_mfma_f32_32x32x16_bf16 v[72:87], v[32:35], v[132:135], v[72:87]\n"
      "v_mfma_f32_32x32x16_bf16 v[88:103], v[36:39], v[132:135], v[88:103]\n"
      "ds_read_b128 v[36:39], v18 offset:20480\n"
      "ds_read_b128 v[32:35], v18 offset:16384\n"
      "s_waitcnt lgkmcnt(6)\n"
      "v_mfma_f32_32x32x16_bf16 v[56:71], v[40:43], v[136:139], v[56:71]\n"
      "v_mfma_f32_32x32x16_bf16 v[104:119], v[44:47], v[136:139], v[104:119]\n"
      "ds_read_b128 v[44:47], v18 offset:26624\n"
      "ds_read_b128 v[40:43], v18 offset:22528\n"
      "s_waitcnt lgkmcnt(4)\n"
      "v_mfma_f32_32x32x16_bf16 v[72:87], v[48:51], v[136:139], v[72:87]\n"
      "v_mfma_f32_32x32x16_bf16 v[88:103], v[52:55], v[136:139], v[88:103]\n"
      "s_barrier\n"
      "ds_read_b64_tr_b16 v[144:145], %[wa] offset:32768\n"
      "ds_read_b64_tr_b16 v[146:147], %[wa] offset:33792\n"
      "ds_read_b128 v[52:55], %[xa] offset:53248\n"
      "ds_read_b128 v[48:51], %[xa] offset:49152\n"
      "s_waitcnt lgkmcnt(6)\n"
      "v_mfma_f32_32x32x16_bf16 v[56:71], v[32:35], v[140:143], v[56:71]\n"
      "v_mfma_f32_32x32x16_bf16 v[104:119], v[36:39], v[140:143], v[104:119]\n"
      "ds_read_b128 v[36:39], %[xa] offset:59392\n"
      "ds_read_b128 v[32:35], %[xa] offset:55296\n"
      "s_waitcnt lgkmcnt(6)\n"
      "v_mfma_f32_32x32x16_bf16 v[72:87], v[40:43], v[140:143], v[72:87]\n"
      "ds_read_b64_tr_b16 v[148:149], %[wa] offset:36864\n"
      "ds_read_b64_tr_b16 v[150:151], %[wa] offset:37888\n"
      "v_mfma_f32_32x32x16_bf16 v[88:103], v[44:47], v[140:143], v[88:103]\n"
      "ds_read_b128 v[44:47], v16 offset:53248\n"
      "ds_read_b128 v[40:43], v16 offset:49152\n"
      "s_waitcnt lgkmcnt(6)\n"
      "v_mfma_f32_32x32x16_bf16 v[56:71], v[48:51], v[144:147], v[56:71]\n"
      "v_mfma_f32_32x32x16_bf16 v[104:119], v[52:55], v[144:147], v[104:119]\n"
      "ds_read_b128 v[52:55], v16 offset:59392\n"
      "ds_read_b128 v[48:51], v16 offset:55296\n"
      "s_waitcnt lgkmcnt(6)\n"
      "v_mfma_f32_32x32x16_bf16 v[72:87], v[32:35], v[144:147], v[72:87]\n"
      "ds_read_b64_tr_b16 v[152:153], %[wa] offset:40960\n"
      "ds_read_b64_tr_b16 v[154:155], %[wa] offset:41984\n"
      "v_mfma_f32_32x32x16_bf16 v[88:103], v[36:39], v[144:147], v[88:103]\n"
      "ds_read_b128 v[36:39], v17 offset:53248\n"
      "ds_read_b128 v[32:35], v17 offset:49152\n"
      "s_waitcnt lgkmcnt(6)\n"
      "v_mfma_f32_32x32x16_bf16 v[56:71], v[40:43], v[148:151], v[56:71]\n"
      "ds_read_b64_tr_b16 v[156:157], %[wa] offset:45056\n"
      "ds_read_b64_tr_b16 v[158:159], %[wa] offset:46080\n"
      "v_mfma_f32_32x32x16_bf16 v[104:119], v[44:47], v[148:151], v[104:119]\n"
      "ds_read_b128 v[44:47], v17 offset:59392\n"
      "ds_read_b128 v[40:43], v17 offset:55296\n"
      "s_waitcnt lgkmcnt(8)\n"
      "v_mfma_f32_32x32x16_bf16 v[72:87], v[48:51], v[148:151], v[72:87]\n"
      "v_mfma_f32_32x32x16_bf16 v[88:103], v[52:55], v[148:151], v[88:103]\n"
      "ds_read_b128 v[52:55], v18 offset:53248\n"
      "ds_read_b128 v[48:51], v18 offset:49152\n"
      "s_waitcnt lgkmcnt(6)\n"
      "v_mfma_f32_32x32x16_bf16 v[56:71], v[32:35], v[152:155], v[56:71]\n"
      "v_mfma_f32_32x32x16_bf16 v[104:119], v[36:39], v[152:155], v[104:119]\n"
      "ds_read_b128 v[36:39], v18 offset:59392\n"
      "ds_read_b128 v[32:35], v18 offset:55296\n"
      "s_waitcnt lgkmcnt(4)\n"
      "v_mfma_f32_32x32x16_bf16 v[72:87], v[40:43], v[152:155], v[72:87]\n"
      "v_mfma_f32_32x32x16_bf16 v[88:103], v[44:47], v[152:155], v[88:103]\n"
      "s_barrier\n"
      "ds_read_b64_tr_b16 v[160:161], v23 offset:0\n"
      "ds_read_b64_tr_b16 v[162:163], v23 offset:1024\n"
      "ds_read_b128 v[44:47], v19 offset:20480\n"
      "ds_read_b128 v[40:43], v19 offset:16384\n"
      "s_waitcnt lgkmcnt(6)\n"
      "v_mfma_f32_32x32x16_bf16 v[56:71], v[48:51], v[156:159], v[56:71]\n"
      "v_mfma_f32_32x32x16_bf16 v[104:119], v[52:55], v[156:159], v[104:119]\n"
      "ds_read_b128 v[52:55], v19 offset:26624\n"
      "ds_read_b128 v[48:51], v19 offset:22528\n"
      "s_waitcnt lgkmcnt(6)\n"
      "v_mfma_f32_32x32x16_bf16 v[72:87], v[32:35], v[156:159], v[72:87]\n"
      "ds_read_b64_tr_b16 v[164:165], v23 offset:4096\n"
      "ds_read_b64_tr_b16 v[166:167], v23 offset:5120\n"
      "v_mfma_f32_32x32x16_bf16 v[88:103], v[36:39], v[156:159], v[88:103]\n"
      "ds_read_b128 v[36:39], v20 offset:20480\n"
      "ds_read_b128 v[32:35], v20 offset:16384\n"
      "s_waitcnt lgkmcnt(6)\n"
      "v_mfma_f32_32x32x16_bf16 v[56:71], v[40:43], v[160:163], v[56:71]\n"
      "v_mfma_f32_32x32x16_bf16 v[104:119], v[44:47], v[160:163], v[104:119]\n"
      "ds_read_b128 v[44:47], v20 offset:26624\n"
      "ds_read_b128 v[40:43], v20 offset:22528\n"
      "s_waitcnt lgkmcnt(6)\n"
      "v_mfma_f32_32x32x16_bf16 v[72:87], v[48:51], v[160:163], v[72:87]\n"
      "ds_read_b64_tr_b16 v[168:169], v23 offset:8192\n"
      "ds_read_b64_tr_b16 v[170:171], v23 offset:9216\n"
      "v_mfma_f32_32x32x16_bf16 v[88:103], v[52:55], v[160:163], v[88:103]\n"
      "ds_read_b128 v[52:55], v21 offset:20480\n"
      "ds_read_b128 v[48:51], v21 offset:16384\n"
      "s_waitcnt lgkmcnt(6)\n"
      "v_mfma_f32_32x32x16_bf16 v[56:71], v[32:35], v[164:167], v[56:71]\n"
      "ds_read_b64_tr_b16 v[172:173], v23 offset:12288\n"
      "ds_read_b64_tr_b16 v[174:175], v23 offset:13312\n"
      "v_mfma_f32_32x32x16_bf16 v[104:119], v[36:39], v[164:167], v[104:119]\n"
      "ds_read_b128 v[36:39], v21 offset:26624\n"
      "ds_read_b128 v[32:35], v21 offset:22528\n"
      "s_waitcnt lgkmcnt(8)\n"
      "v_mfma_f32_32x32x16_bf16 v[72:87], v[40:43], v[164:167], v[72:87]\n"
      "v_mfma_f32_32x32x16_bf16 v[88:103], v[44:47], v[164:167], v[88:103]\n"
      "ds_read_b128 v[44:47], v22 offset:20480\n"
      "ds_read_b128 v[40:43], v22 offset:16384\n"
      "s_waitcnt lgkmcnt(6)\n"
      "v_mfma_f32_32x32x16_bf16 v[56:71], v[48:51], v[168:171], v[56:71]\n"
      "v_mfma_f32_32x32x16_bf16 v[104:119], v[52:55], v[168:171], v[104:119]\n"
      "ds_read_b128 v[52:55], v22 offset:26624\n"
      "ds_read_b128 v[48:51], v22 offset:22528\n"
      "s_waitcnt lgkmcnt(4)\n"
      "v_mfma_f32_32x32x16_bf16 v[72:87], v[32:35], v[168:171], v[72:87]\n"
      "v_mfma_f32_32x32x16_bf16 v[88:103], v[36:39], v[168:171], v[88:103]\n"
      "s_barrier\n"
      "ds_read_b64_tr_b16 v[176:177], v23 offset:32768\n"
      "ds_read_b64_tr_b16 v[178:179], v23 offset:33792\n"
      "ds_read_b128 v[36:39], v19 offset:53248\n"
      "ds_read_b128 v[32:35], v19 offset:49152\n"
      "s_waitcnt lgkmcnt(6)\n"
      "v_mfma_f32_32x32x16_bf16 v[56:71], v[40:43], v[172:175], v[56:71]\n"
      "v_mfma_f32_32x32x16_bf16 v[104:119], v[44:47], v[172:175], v[104:119]\n"
      "ds_read_b128 v[44:47], v19 offset:59392\n"
      "ds_read_b128 v[40:43], v19 offset:55296\n"
      "s_waitcnt lgkmcnt(6)\n"
      "v_mfma_f32_32x32x16_bf16 v[72:87], v[48:51], v[172:175], v[72:87]\n"
      "ds_read_b64_tr_b16 v[180:181], v23 offset:36864\n"
      "ds_read_b64_tr_b16 v[182:183], v23 offset:37888\n"
      "v_mfma_f32_32x32x16_bf16 v[88:103], v[52:55], v[172:175], v[88:103]\n"
      "ds_read_b128 v[52:55], v20 offset:53248\n"
      "ds_read_b128 v[48:51], v20 offset:49152\n"
      "s_waitcnt lgkmcnt(6)\n"
      "v_mfma_f32_32x32x16_bf16 v[56:71], v[32:35], v[176:179], v[56:71]\n"
      "v_mfma_f32_32x32x16_bf16 v[104:119], v[36:39], v[176:179], v[104:119]\n"
      "ds_read_b128 v[36:39], v20 offset:59392\n"
      "ds_read_b128 v[32:35], v20 offset:55296\n"
      "s_waitcnt lgkmcnt(6)\n"
      "v_mfma_f32_32x32x16_bf16 v[72:87], v[40:43], v[176:179], v[72:87]\n"
      "ds_read_b64_tr_b16 v[184:185], v23 offset:40960\n"
      "ds_read_b64_tr_b16 v[186:187], v23 offset:41984\n"
      "v_mfma_f32_32x32x16_bf16 v[88:103], v[44:47], v[176:179], v[88:103]\n"
      "ds_read_b128 v[44:47], v21 offset:53248\n"
      "ds_read_b128 v[40:43], v21 offset:49152\n"
      "s_waitcnt lgkmcnt(6)\n"
      "v_mfma_f32_32x32x16_bf16 v[56:71], v[48:51], v[180:183], v[56:71]\n"
      "ds_read_b64_tr_b16 v[188:189], v23 offset:45056\n"
      "ds_read_b64_tr_b16 v[190:191], v23 offset:46080\n"
      "v_mfma_f32_32x32x16_bf16 v[104:119], v[52:55], v[180:183], v[104:119]\n"
      "ds_read_b128 v[52:55], v21 offset:59392\n"
      "ds_read_b128 v[48:51], v21 offset:55296\n"
      "s_waitcnt lgkmcnt(8)\n"
      "v_mfma_f32_32x32x16_bf16 v[72:87], v[32:35], v[180:183], v[72:87]\n"
      "v_mfma_f32_32x32x16_bf16 v[88:103], v[36:39], v[180:183], v[88:103]\n"
      "ds_read_b128 v[36:39], v22 offset:53248\n"
      "ds_read_b128 v[32:35], v22 offset:49152\n"
      "s_waitcnt lgkmcnt(6)\n"
      "v_mfma_f32_32x32x16_bf16 v[56:71], v[40:43], v[184:187], v[56:71]\n"
      "v_mfma_f32_32x32x16_bf16 v[104:119], v[44:47], v[184:187], v[104:119]\n"
      "ds_read_b128 v[44:47], v22 offset:59392\n"
      "ds_read_b128 v[40:43], v22 offset:55296\n"
      "s_waitcnt lgkmcnt(4)\n"
      "v_mfma_f32_32x32x16_bf16 v[72:87], v[48:51], v[184:187], v[72:87]\n"
      "v_mfma_f32_32x32x16_bf16 v[88:103], v[52:55], v[184:187], v[88:103]\n"
      "s_barrier\n"
      "ds_read_b64_tr_b16 v[192:193], %[wa] offset:0\n"
      "ds_read_b64_tr_b16 v[194:195], %[wa] offset:1024\n"
      "ds_read_b128 v[52:55], %[xa] offset:20480\n"
      "ds_read_b128 v[48:51], %[xa] offset:16384\n"
      "s_waitcnt lgkmcnt(6)\n"
      "v_mfma_f32_32x32x16_bf16 v[56:71], v[32:35], v[188:191], v[56:71]\n"
      "v_mfma_f32_32x32x16_bf16 v[104:119], v[36:39], v[188:191], v[104:119]\n"
      "ds_read_b128 v[36:39], %[xa] offset:26624\n"
      "ds_read_b128 v[32:35], %[xa] offset:22528\n"
      "s_waitcnt lgkmcnt(6)\n"
      "v_mfma_f32_32x32x16_bf16 v[72:87], v[40:43], v[188:191], v[72:87]\n"
      "ds_read_b64_tr_b16 v[196:197], %[wa] offset:4096\n"
      "ds_read_b64_tr_b16 v[198:199], %[wa] offset:5120\n"
      "v_mfma_f32_32x32x16_bf16 v[88:103], v[44:47], v[188:191], v[88:103]\n"
      "ds_read_b128 v[44:47], v16 offset:20480\n"
      "ds_read_b128 v[40:43], v16 offset:16384\n"
      "s_waitcnt lgkmcnt(6)\n"
      "v_mfma_f32_32x32x16_bf16 v[56:71], v[48:51], v[192:195], v[56:71]\n"
      "v_mfma_f32_32x32x16_bf16 v[104:119], v[52:55], v[192:195], v[104:119]\n"
      "ds_read_b128 v[52:55], v16 offset:26624\n"
      "ds_read_b128 v[48:51], v16 offset:22528\n"
      "s_waitcnt lgkmcnt(6)\n"
      "v_mfma_f32_32x32x16_bf16 v[72:87], v[32:35], v[192:195], v[72:87]\n"
      "ds_read_b64_tr_b16 v[200:201], %[wa] offset:8192\n"
      "ds_read_b64_tr_b16 v[202:203], %[wa] offset:9216\n"
      "v_mfma_f32_32x32x16_bf16 v[88:103], v[36:39], v[192:195], v[88:103]\n"
      "ds_read_b128 v[36:39], v17 offset:20480\n"
      "ds_read_b128 v[32:35], v17 offset:16384\n"
      "s_waitcnt lgkmcnt(6)\n"
      "v_mfma_f32_32x32x16_bf16 v[56:71], v[40:43], v[196:199], v[56:71]\n"
      "ds_read_b64_tr_b16 v[204:205], %[wa] offset:12288\n"
      "ds_read_b64_tr_b16 v[206:207], %[wa] offset:13312\n"
      "v_mfma_f32_32x32x16_bf16 v[104:119], v[44:47], v[196:199], v[104:119]\n"
      "ds_read_b128 v[44:47], v17 offset:26624\n"
      "ds_read_b128 v[40:43], v17 offset:22528\n"
      "s_waitcnt lgkmcnt(8)\n"
      "v_mfma_f32_32x32x16_bf16 v[72:87], v[48:51], v[196:199], v[72:87]\n"
      "v_mfma_f32_32x32x16_bf16 v[88:103], v[52:55], v[196:199], v[88:103]\n"
      "ds_read_b128 v[52:55], v18 offset:20480\n"
      "ds_read_b128 v[48:51], v18 offset:16384\n"
      "s_waitcnt lgkmcnt(6)\n"
      "v_mfma_f32_32x32x16_bf16 v[56:71], v[32:35], v[200:203], v[56:71]\n"
      "v_mfma_f32_32x32x16_bf16 v[104:119], v[36:39], v[200:203], v[104:119]\n"
      "ds_read_b128 v[36:39], v18 offset:26624\n"
      "ds_read_b128 v[32:35], v18 offset:22528\n"
      "s_waitcnt lgkmcnt(4)\n"
      "v_mfma_f32_32x32x16_bf16 v[72:87], v[40:43], v[200:203], v[72:87]\n"
      "v_mfma_f32_32x32x16_bf16 v[88:103], v[44:47], v[200:203], v[88:103]\n"
      "s_barrier\n"
      "ds_read_b64_tr_b16 v[208:209], %[wa] offset:32768\n"
      "ds_read_b64_tr_b16 v[210:211], %[wa] offset:33792\n"
      "ds_read_b128 v[44:47], %[xa] offset:53248\n"
      "ds_read_b128 v[40:43], %[xa] offset:49152\n"
      "s_waitcnt lgkmcnt(6)\n"
      "v_mfma_f32_32x32x16_bf16 v[56:71], v[48:51], v[204:207], v[56:71]\n"
      "v_mfma_f32_32x32x16_bf16 v[104:119], v[52:55], v[204:207], v[104:119]\n"
      "ds_read_b128 v[52:55], %[xa] offset:59392\n"
      "ds_read_b128 v[48:51], %[xa] offset:55296\n"
      "s_waitcnt lgkmcnt(6)\n"
      "v_mfma_f32_32x32x16_bf16 v[72:87], v[32:35], v[204:207], v[72:87]\n"
      "ds_read_b64_tr_b16 v[212:213], %[wa] offset:36864\n"
      "ds_read_b64_tr_b16 v[214:215], %[wa] offset:37888\n"
      "v_mfma_f32_32x32x16_bf16 v[88:103], v[36:39], v[204:207], v[88:103]\n"
      "ds_read_b128 v[36:39], v16 offset:53248\n"
      "ds_read_b128 v[32:35], v16 offset:49152\n"
      "s_waitcnt lgkmcnt(6)\n"
      "v_mfma_f32_32x32x16_bf16 v[56:71], v[40:43], v[208:211], v[56:71]\n"
      "v_mfma_f32_32x32x16_bf16 v[104:119], v[44:47], v[208:211], v[104:119]\n"
      "ds_read_b128 v[44:47], v16 offset:59392\n"
      "ds_read_b128 v[40:43], v16 offset:55296\n"
      "s_waitcnt lgkmcnt(6)\n"
      "v_mfma_f32_32x32x16_bf16 v[72:87], v[48:51], v[208:211], v[72:87]\n"
      "ds_read_b64_tr_b16 v[216:217], %[wa] offset:40960\n"
      "ds_read_b64_tr_b16 v[218:219], %[wa] offset:41984\n"
      "v_mfma_f32_32x32x16_bf16 v[88:103], v[52:55], v[208:211], v[88:103]\n"
      "ds_read_b128 v[52:55], v17 offset:53248\n"
      "ds_read_b128 v[48:51], v17 offset:49152\n"
      "s_waitcnt lgkmcnt(6)\n"
      "v_mfma_f32_32x32x16_bf16 v[56:71], v[32:35], v[212:215], v[56:71]\n"
      "ds_read_b64_tr_b16 v[220:221], %[wa] offset:45056\n"
      "ds_read_b64_tr_b16 v[222:223], %[wa] offset:46080\n"
      "v_mfma_f32_32x32x16_bf16 v[104:119], v[36:39], v[212:215], v[104:119]\n"
      "ds_read_b128 v[36:39], v17 offset:59392\n"
      "ds_read_b128 v[32:35], v17 offset:55296\n"
      "s_waitcnt lgkmcnt(8)\n"
      "v_mfma_f32_32x32x16_bf16 v[72:87], v[40:43], v[212:215], v[72:87]\n"
      "v_mfma_f32_32x32x16_bf16 v[88:103], v[44:47], v[212:215], v[88:103]\n"
      "ds_read_b128 v[44:47], v18 offset:53248\n"
      "ds_read_b128 v[40:43], v18 offset:49152\n"
      "s_waitcnt lgkmcnt(6)\n"
      "v_mfma_f32_32x32x16_bf16 v[56:71], v[48:51], v[216:219], v[56:71]\n"
      "v_mfma_f32_32x32x16_bf16 v[104:119], v[52:55], v[216:219], v[104:119]\n"
      "ds_read_b128 v[52:55], v18 offset:59392\n"
      "ds_read_b128 v[48:51], v18 offset:55296\n"
      "s_waitcnt lgkmcnt(4)\n"
      "v_mfma_f32_32x32x16_bf16 v[72:87], v[32:35], v[216:219], v[72:87]\n"
      "v_mfma_f32_32x32x16_bf16 v[88:103], v[36:39], v[216:219], v[88:103]\n"
      "s_barrier\n"
      "ds_read_b64_tr_b16 v[224:225], v23 offset:0\n"
      "ds_read_b64_tr_b16 v[226:227], v23 offset:1024\n"
      "ds_read_b128 v[36:39], v19 offset:20480\n"
      "ds_read_b128 v[32:35], v19 offset:16384\n"
      "s_waitcnt lgkmcnt(6)\n"
      "v_mfma_f32_32x32x16_bf16 v[56:71], v[40:43], v[220:223], v[56:71]\n"
      "v_mfma_f32_32x32x16_bf16 v[104:119], v[44:47], v[220:223], v[104:119]\n"
      "ds_read_b128 v[44:47], v19 offset:26624\n"
      "ds_read_b128 v[40:43], v19 offset:22528\n"
      "s_waitcnt lgkmcnt(6)\n"
      "v_mfma_f32_32x32x16_bf16 v[72:87], v[48:51], v[220:223], v[72:87]\n"
      "ds_read_b64_tr_b16 v[228:229], v23 offset:4096\n"
      "ds_read_b64_tr_b16 v[230:231], v23 offset:5120\n"
      "v_mfma_f32_32x32x16_bf16 v[88:103], v[52:55], v[220:223], v[88:103]\n"
      "ds_read_b128 v[52:55], v20 offset:20480\n"
      "ds_read_b128 v[48:51], v20 offset:16384\n"
      "s_waitcnt lgkmcnt(6)\n"
      "v_mfma_f32_32x32x16_bf16 v[56:71], v[32:35], v[224:227], v[56:71]\n"
      "v_mfma_f32_32x32x16_bf16 v[104:119], v[36:39], v[224:227], v[104:119]\n"
      "ds_read_b128 v[36:39], v20 offset:26624\n"
      "ds_read_b128 v[32:35], v20 offset:22528\n"
      "s_waitcnt lgkmcnt(6)\n"
      "v_mfma_f32_32x32x16_bf16 v[72:87], v[40:43], v[224:227], v[72:87]\n"
      "ds_read_b64_tr_b16 v[232:233], v23 offset:8192\n"
      "ds_read_b64_tr_b16 v[234:235], v23 offset:9216\n"
      "v_mfma_f32_32x32x16_bf16 v[88:103], v[44:47], v[224:227], v[88:103]\n"
      "ds_read_b128 v[44:47], v21 offset:20480\n"
      "ds_read_b128 v[40:43], v21 offset:16384\n"
      "s_waitcnt lgkmcnt(6)\n"
      "v_mfma_f32_32x32x16_bf16 v[56:71], v[48:51], v[228:231], v[56:71]\n"
      "ds_read_b64_tr_b16 v[236:237], v23 offset:12288\n"
      "ds_read_b64_tr_b16 v[238:239], v23 offset:13312\n"
      "v_mfma_f32_32x32x16_bf16 v[104:119], v[52:55], v[228:231], v[104:119]\n"
      "ds_read_b128 v[52:55], v21 offset:26624\n"
      "ds_read_b128 v[48:51], v21 offset:22528\n"
      "s_waitcnt lgkmcnt(8)\n"
      "v_mfma_f32_32x32x16_bf16 v[72:87], v[32:35], v[228:231], v[72:87]\n"
      "v_mfma_f32_32x32x16_bf16 v[88:103], v[36:39], v[228:231], v[88:103]\n"
      "ds_read_b128 v[36:39], v22 offset:20480\n"
      "ds_read_b128 v[32:35], v22 offset:16384\n"
      "s_waitcnt lgkmcnt(6)\n"
      "v_mfma_f32_32x32x16_bf16 v[56:71], v[40:43], v[232:235], v[56:71]\n"
      "v_mfma_f32_32x32x16_bf16 v[104:119], v[44:47], v[232:235], v[104:119]\n"
      "ds_read_b128 v[44:47], v22 offset:26624\n"
      "ds_read_b128 v[40:43], v22 offset:22528\n"
      "s_waitcnt lgkmcnt(4)\n"
      "v_mfma_f32_32x32x16_bf16 v[72:87], v[48:51], v[232:235], v[72:87]\n"
      "v_mfma_f32_32x32x16_bf16 v[88:103], v[52:55], v[232:235], v[88:103]\n"
      "s_barrier\n"
      "ds_read_b64_tr_b16 v[240:241], v23 offset:32768\n"
      "ds_read_b64_tr_b16 v[242:243], v23 offset:33792\n"
      "ds_read_b128 v[52:55], v20 offset:49152\n"
      "ds_read_b128 v[48:51], v19 offset:49152\n"
      "s_waitcnt lgkmcnt(6)\n"
      "v_mfma_f32_32x32x16_bf16 v[56:71], v[32:35], v[236:239], v[56:71]\n"
      "ds_read_b64_tr_b16 v[244:245], v23 offset:36864\n"
      "ds_read_b64_tr_b16 v[246:247], v23 offset:37888\n"
      "v_mfma_f32_32x32x16_bf16 v[104:119], v[36:39], v[236:239], v[104:119]\n"
      "ds_read_b64_tr_b16 v[248:249], v23 offset:40960\n"
      "ds_read_b64_tr_b16 v[250:251], v23 offset:41984\n"
      "ds_read_b128 v[36:39], v22 offset:49152\n"
      "ds_read_b128 v[32:35], v21 offset:49152\n"
      "s_waitcnt lgkmcnt(10)\n"
      "v_mfma_f32_32x32x16_bf16 v[72:87], v[40:43], v[236:239], v[72:87]\n"
      "ds_read_b64_tr_b16 v[252:253], v23 offset:45056\n"
      "ds_read_b64_tr_b16 v[254:255], v23 offset:46080\n"
      "v_mfma_f32_32x32x16_bf16 v[88:103], v[44:47], v[236:239], v[88:103]\n"
      "ds_read_b128 v[44:47], v20 offset:53248\n"
      "ds_read_b128 v[40:43], v19 offset:53248\n"
      "s_waitcnt lgkmcnt(10)\n"
      "v_mfma_f32_32x32x16_bf16 v[56:71], v[48:51], v[240:243], v[56:71]\n"
      "s_waitcnt lgkmcnt(8)\n"
      "v_mfma_f32_32x32x16_bf16 v[56:71], v[52:55], v[244:247], v[56:71]\n"
      "ds_read_b128 v[52:55], v22 offset:53248\n"
      "ds_read_b128 v[48:51], v21 offset:53248\n"
      "s_waitcnt lgkmcnt(6)\n"
      "v_mfma_f32_32x32x16_bf16 v[56:71], v[32:35], v[248:251], v[56:71]\n"
      "s_waitcnt lgkmcnt(4)\n"
      "v_mfma_f32_32x32x16_bf16 v[56:71], v[36:39], v[252:255], v[56:71]\n"
      "ds_read_b128 v[36:39], v20 offset:55296\n"
      "ds_read_b128 v[32:35], v19 offset:55296\n"
      "s_waitcnt lgkmcnt(4)\n"
      "v_mfma_f32_32x32x16_bf16 v[104:119], v[40:43], v[240:243], v[104:119]\n"
      "v_mfma_f32_32x32x16_bf16 v[104:119], v[44:47], v[244:247], v[104:119]\n"
      "ds_read_b128 v[44:47], v22 offset:55296\n"
      "ds_read_b128 v[40:43], v21 offset:55296\n"
      "s_waitcnt lgkmcnt(4)\n"
      "v_mfma_f32_32x32x16_bf16 v[104:119], v[48:51], v[248:251], v[104:119]\n"
      "v_mfma_f32_32x32x16_bf16 v[104:119], v[52:55], v[252:255], v[104:119]\n"
      "ds_read_b128 v[52:55], v20 offset:59392\n"
      "ds_read_b128 v[48:51], v19 offset:59392\n"
      "s_nop 0\n"
      "v_permlane16_swap_b32 v56, v64\n"
      "s_waitcnt lgkmcnt(4)\n"
      "v_mfma_f32_32x32x16_bf16 v[72:87], v[32:35], v[240:243], v[72:87]\n"
      "v_mul_f32 v64, s90, v64\n"
      "v_permlane16_swap_b32 v57, v65\n"
      "v_permlane16_swap_b32 v58, v66\n"
      "v_mfma_f32_32x32x16_bf16 v[72:87], v[36:39], v[244:247], v[72:87]\n"
      "ds_read_b128 v[36:39], v22 offset:59392\n"
      "ds_read_b128 v[32:35], v21 offset:59392\n"
      "v_exp_f32 v64, v64\n"
      "v_mul_f32 v65, s90, v65\n"
      "s_waitcnt lgkmcnt(4)\n"
      "v_mfma_f32_32x32x16_bf16 v[72:87], v[40:43], v[248:251], v[72:87]\n"
      "v_add_f32 v64, 1.0, v64\n"
      "v_exp_f32 v65, v65\n"
      "v_mul_f32 v66, s90, v66\n"
      "v_permlane16_swap_b32 v59, v67\n"
      "v_mfma_f32_32x32x16_bf16 v[72:87], v[44:47], v[252:255], v[72:87]\n"
      "s_barrier\n"
      "ds_read_b128 v[44:47], v16 offset:0\n"
      "ds_read_b128 v[40:43], %[xa] offset:0\n"
      "v_rcp_f32 v64, v64\n"
      "s_waitcnt lgkmcnt(4)\n"
      "v_mfma_f32_32x32x16_bf16 v[88:103], v[48:51], v[240:243], v[88:103]\n"
      "v_mul_f32 v56, v56, v64\n"
      "v_add_f32 v65, 1.0, v65\n"
      "v_exp_f32 v66, v66\n"
      "v_mul_f32 v67, s90, v67\n"
      "v_mfma_f32_32x32x16_bf16 v[88:103], v[52:55], v[244:247], v[88:103]\n"
      "ds_read_b128 v[52:55], v18 offset:0\n"
      "ds_read_b128 v[48:51], v17 offset:0\n"
      "v_rcp_f32 v65, v65\n"
      "v_add_f32 v66, 1.0, v66\n"
      "s_waitcnt lgkmcnt(4)\n"
      "v_mfma_f32_32x32x16_bf16 v[88:103], v[32:35], v[248:251], v[88:103]\n"
      "v_mul_f32 v57, v57, v65\n"
      "v_rcp_f32 v66, v66\n"
      "v_exp_f32 v67, v67\n"
      "v_mfma_f32_32x32x16_bf16 v[88:103], v[36:39], v[252:255], v[88:103]\n"
      "ds_read_b128 v[36:39], v16 offset:4096\n"
      "ds_read_b128 v[32:35], %[xa] offset:4096\n"
      "v_cvt_pk_bf16_f32 v64, v56, v57\n"
      "v_mul_f32 v58, v58, v66\n"
      "s_waitcnt lgkmcnt(4)\n"
      "v_mfma_f32_32x32x16_bf16 v[112:127], v[40:43], v[128:131], 0\n"
      "v_add_f32 v67, 1.0, v67\n"
      "v_permlane16_swap_b32 v60, v68\n"
      "v_permlane16_swap_b32 v61, v69\n"
      "v_mfma_f32_32x32x16_bf16 v[112:127], v[44:47], v[132:135], v[112:127]\n"
      "ds_read_b128 v[44:47], v18 offset:4096\n"
      "ds_read_b128 v[40:43], v17 offset:4096\n"
      "v_rcp_f32 v67, v67\n"
      "v_mul_f32 v68, s90, v68\n"
      "s_waitcnt lgkmcnt(4)\n"
      "v_mfma_f32_32x32x16_bf16 v[112:127], v[48:51], v[136:139], v[112:127]\n"
      "v_mul_f32 v59, v59, v67\n"
      "v_exp_f32 v68, v68\n"
      "v_mul_f32 v69, s90, v69\n"
      "v_permlane16_swap_b32 v62, v70\n"
      "v_mfma_f32_32x32x16_bf16 v[112:127], v[52:55], v[140:143], v[112:127]\n"
      "ds_read_b128 v[52:55], v16 offset:8192\n"
      "ds_read_b128 v[48:51], %[xa] offset:8192\n"
      "v_cvt_pk_bf16_f32 v65, v58, v59\n"
      "v_add_f32 v68, 1.0, v68\n"
      "s_waitcnt lgkmcnt(4)\n"
      "v_mfma_f32_32x32x16_bf16 v[112:127], v[32:35], v[144:147], v[112:127]\n"
      "v_rcp_f32 v68, v68\n"
      "v_exp_f32 v69, v69\n"
      "v_mul_f32 v70, s90, v70\n"
      "v_mfma_f32_32x32x16_bf16 v[112:127], v[36:39], v[148:151], v[112:127]\n"
      "ds_read_b128 v[36:39], v18 offset:8192\n"
      "ds_read_b128 v[32:35], v17 offset:8192\n"
      "v_mul_f32 v60, v60, v68\n"
      "v_add_f32 v69, 1.0, v69\n"
      "s_waitcnt lgkmcnt(4)\n"
      "v_mfma_f32_32x32x16_bf16 v[112:127], v[40:43], v[152:155], v[112:127]\n"
      "v_rcp_f32 v69, v69\n"
      "v_exp_f32 v70, v70\n"
      "v_permlane16_swap_b32 v63, v71\n"
      "v_mfma_f32_32x32x16_bf16 v[112:127], v[44:47], v[156:159], v[112:127]\n"
      "ds_read_b128 v[44:47], v16 offset:12288\n"
      "ds_read_b128 v[40:43], %[xa] offset:12288\n"
      "ds_read_b32 v28, %[fa]\n"
      "v_mul_f32 v61, v61, v69\n"
      "s_waitcnt lgkmcnt(5)\n"
      "v_mfma_f32_32x32x16_bf16 v[112:127], v[48:51], v[160:163], v[112:127]\n"
      "v_cvt_pk_bf16_f32 v66, v60, v61\n"
      "v_add_f32 v70, 1.0, v70\n"
      "v_mul_f32 v71, s90, v71\n"
      "v_permlane16_swap_b32 v104, v108\n"
      "v_mfma_f32_32x32x16_bf16 v[112:127], v[52:55], v[164:167], v[112:127]\n"
      "ds_read_b128 v[52:55], v18 offset:12288\n"
      "ds_read_b128 v[48:51], v17 offset:12288\n"
      "v_rcp_f32 v70, v70\n"
      "v_mul_f32 v108, s90, v108\n"
      "s_waitcnt lgkmcnt(5)\n"
      "v_mfma_f32_32x32x16_bf16 v[112:127], v[32:35], v[168:171], v[112:127]\n"
      "v_cmp_gt_u32 vcc, 1, v28\n"
      "v_mul_f32 v62, v62, v70\n"
      "v_exp_f32 v71, v71\n"
      "v_exp_f32 v108, v108\n"
      "v_mfma_f32_32x32x16_bf16 v[112:127], v[36:39], v[172:175], v[112:127]\n"
      "s_cbranch_vccnz .LsT0b_%=\n"
      ".LrT0b_%=:\n"
      "ds_read_b128 v[36:39], v16 offset:16384\n"
      "ds_read_b128 v[32:35], %[xa] offset:16384\n"
      "v_add_f32 v71, 1.0, v71\n"
      "s_waitcnt lgkmcnt(5)\n"
      "v_mfma_f32_32x32x16_bf16 v[112:127], v[40:43], v[176:179], v[112:127]\n"
      "v_rcp_f32 v71, v71\n"
      "v_add_f32 v108, 1.0, v108\n"
      "v_permlane16_swap_b32 v105, v109\n"
      "v_mfma_f32_32x32x16_bf16 v[112:127], v[44:47], v[180:183], v[112:127]\n"
      "ds_read_b128 v[44:47], v18 offset:16384\n"
      "ds_read_b128 v[40:43], v17 offset:16384\n"
      "v_mul_f32 v63, v63, v71\n"
      "v_rcp_f32 v108, v108\n"
      "s_waitcnt lgkmcnt(4)\n"
      "v_mfma_f32_32x32x16_bf16 v[112:127], v[48:51], v[184:187], v[112:127]\n"
      "v_cvt_pk_bf16_f32 v67, v62, v63\n"
      "s_mov_b64 exec, %[mf]\n"
      "v_mov_b32 v64, 0\n"
      "v_mov_b32 v65, 0\n"
      "v_mov_b32 v66, 0\n"
      "v_and_b32 v67, 0xffff0000, v67\n"
      "s_mov_b64 exec, -1\n"
      "v_mfma_f32_32x32x16_bf16 v[112:127], v[52:55], v[188:191], v[112:127]\n"
      "ds_read_b128 v[52:55], v16 offset:20480\n"
      "ds_read_b128 v[48:51], %[xa] offset:20480\n"
      "v_mul_f32 v104, v104, v108\n"
      "v_mul_f32 v109, s90, v109\n"
      "s_waitcnt lgkmcnt(4)\n"
      "v_mfma_f32_32x32x16_bf16 v[112:127], v[32:35], v[192:195], v[112:127]\n"
      "v_exp_f32 v109, v109\n"
      "v_permlane16_swap_b32 v106, v110\n"
      "v_permlane16_swap_b32 v107, v111\n"
      "v_mfma_f32_32x32x16_bf16 v[112:127], v[36:39], v[196:199], v[112:127]\n"
      "ds_read_b128 v[36:39], v18 offset:20480\n"
      "ds_read_b128 v[32:35], v17 offset:20480\n"
      "v_add_f32 v109, 1.0, v109\n"
      "v_mul_f32 v110, s90, v110\n"
      "s_waitcnt lgkmcnt(4)\n"
      "v_mfma_f32_32x32x16_bf16 v[112:127], v[40:43], v[200:203], v[112:127]\n"
      "v_rcp_f32 v109, v109\n"
      "v_exp_f32 v110, v110\n"
      "v_mul_f32 v111, s90, v111\n"
      "v_mfma_f32_32x32x16_bf16 v[112:127], v[44:47], v[204:207], v[112:127]\n"
      "ds_read_b128 v[44:47], v16 offset:24576\n"
      "ds_read_b128 v[40:43], %[xa] offset:24576\n"
      "v_mul_f32 v105, v105, v109\n"
      "v_add_f32 v110, 1.0, v110\n"
      "s_waitcnt lgkmcnt(4)\n"
      "v_mfma_f32_32x32x16_bf16 v[112:127], v[48:51], v[208:211], v[112:127]\n"
      "v_cvt_pk_bf16_f32 v108, v104, v105\n"
      "v_rcp_f32 v110, v110\n"
      "v_exp_f32 v111, v111\n"
      "v_mfma_f32_32x32x16_bf16 v[112:127], v[52:55], v[212:215], v[112:127]\n"
      "ds_read_b128 v[52:55], v18 offset:24576\n"
      "ds_read_b128 v[48:51], v17 offset:24576\n"
      "v_mul_f32 v106, v106, v110\n"
      "v_add_f32 v111, 1.0, v111\n"
      "s_waitcnt lgkmcnt(4)\n"
      "v_mfma_f32_32x32x16_bf16 v[112:127], v[32:35], v[216:219], v[112:127]\n"
      "v_rcp_f32 v111, v111\n"
      "v_permlane16_swap_b32 v72, v80\n"
      "v_permlane16_swap_b32 v73, v81\n"
      "v_mfma_f32_32x32x16_bf16 v[112:127], v[36:39], v[220:223], v[112:127]\n"
      "ds_read_b128 v[36:39], v16 offset:28672\n"
      "ds_read_b128 v[32:35], %[xa] offset:28672\n"
      "ds_read_b32 v28, %[fa]\n"
      "v_mul_f32 v107, v107, v111\n"
      "s_waitcnt lgkmcnt(5)\n"
      "v_mfma_f32_32x32x16_bf16 v[112:127], v[40:43], v[224:227], v[112:127]\n"
      "v_cvt_pk_bf16_f32 v109, v106, v107\n"
      "ds_write_b64 %[hh], v[108:109] offset:64\n"
      "ds_add_u32 %[sg], v27\n"
      "v_mul_f32 v80, s90, v80\n"
      "v_mul_f32 v81, s90, v81\n"
      "v_mfma_f32_32x32x16_bf16 v[112:127], v[44:47], v[228:231], v[112:127]\n"
      "ds_read_b128 v[44:47], v18 offset:28672\n"
      "ds_read_b128 v[40:43], v17 offset:28672\n"
      "v_exp_f32 v80, v80\n"
      "s_waitcnt lgkmcnt(7)\n"
      "v_mfma_f32_32x32x16_bf16 v[112:127], v[48:51], v[232:235], v[112:127]\n"
      "v_cmp_gt_u32 vcc, 2, v28\n"
      "ds_write_b128 %[ha], v[64:67] offset:0\n"
      "ds_add_u32 %[sg], v27\n"
      "v_add_f32 v80, 1.0, v80\n"
      "v_exp_f32 v81, v81\n"
      "v_mfma_f32_32x32x16_bf16 v[112:127], v[52:55], v[236:239], v[112:127]\n"
      "s_cbranch_vccnz .LsT1a_%=\n"
      ".LrT1a_%=:\n"
      "ds_read_b128 v[52:55], v16 offset:32768\n"
      "ds_read_b128 v[48:51], %[xa] offset:32768\n"
      "v_rcp_f32 v80, v80\n"
      "s_waitcnt lgkmcnt(9)\n"
      "v_mfma_f32_32x32x16_bf16 v[112:127], v[32:35], v[240:243], v[112:127]\n"
      "v_mul_f32 v72, v72, v80\n"
      "v_add_f32 v81, 1.0, v81\n"
      "v_permlane16_swap_b32 v74, v82\n"
      "v_permlane16_swap_b32 v75, v83\n"
      "v_mfma_f32_32x32x16_bf16 v[112:127], v[36:39], v[244:247], v[112:127]\n"
      "ds_read_b128 v[36:39], v18 offset:32768\n"
      "ds_read_b128 v[32:35], v17 offset:32768\n"
      "v_rcp_f32 v81, v81\n"
      "v_mul_f32 v82, s90, v82\n"
      "s_waitcnt lgkmcnt(6)\n"
      "v_mfma_f32_32x32x16_bf16 v[112:127], v[40:43], v[248:251], v[112:127]\n"
      "v_mul_f32 v73, v73, v81\n"
      "v_exp_f32 v82, v82\n"
      "v_mul_f32 v83, s90, v83\n"
      "v_permlane16_swap_b32 v76, v84\n"
      "v_mfma_f32_32x32x16_bf16 v[112:127], v[44:47], v[252:255], v[112:127]\n"
      "ds_read_b128 v[44:47], v16 offset:36864\n"
      "ds_read_b128 v[40:43], %[xa] offset:36864\n"
      "v_cvt_pk_bf16_f32 v80, v72, v73\n"
      "v_add_f32 v82, 1.0, v82\n"
      "s_waitcnt lgkmcnt(4)\n"
      "v_mfma_f32_32x32x16_bf16 v[56:71], v[48:51], v[128:131], 0\n"
      "v_rcp_f32 v82, v82\n"
      "v_exp_f32 v83, v83\n"
      "v_mul_f32 v84, s90, v84\n"
      "v_mfma_f32_32x32x16_bf16 v[56:71], v[52:55], v[132:135], v[56:71]\n"
      "ds_read_b128 v[52:55], v18 offset:36864\n"
      "ds_read_b128 v[48:51], v17 offset:36864\n"
      "v_mul_f32 v74, v74, v82\n"
      "v_add_f32 v83, 1.0, v83\n"
      "s_waitcnt lgkmcnt(4)\n"
      "v_mfma_f32_32x32x16_bf16 v[56:71], v[32:35], v[136:139], v[56:71]\n"
      "v_rcp_f32 v83, v83\n"
      "v_exp_f32 v84, v84\n"
      "v_permlane16_swap_b32 v77, v85\n"
      "v_mfma_f32_32x32x16_bf16 v[56:71], v[36:39], v[140:143], v[56:71]\n"
      "ds_read_b128 v[36:39], v16 offset:40960\n"
      "ds_read_b128 v[32:35], %[xa] offset:40960\n"
      "v_mul_f32 v75, v75, v83\n"
      "v_add_f32 v84, 1.0, v84\n"
      "s_waitcnt lgkmcnt(4)\n"
      "v_mfma_f32_32x32x16_bf16 v[56:71], v[40:43], v[144:147], v[56:71]\n"
      "v_cvt_pk_bf16_f32 v81, v74, v75\n"
      "v_rcp_f32 v84, v84\n"
      "v_mul_f32 v85, s90, v85\n"
      "v_permlane16_swap_b32 v78, v86\n"
      "v_mfma_f32_32x32x16_bf16 v[56:71], v[44:47], v[148:151], v[56:71]\n"
      "ds_read_b128 v[44:47], v18 offset:40960\n"
      "ds_read_b128 v[40:43], v17 offset:40960\n"
      "v_mul_f32 v76, v76, v84\n"
      "v_exp_f32 v85, v85\n"
      "s_waitcnt lgkmcnt(4)\n"
      "v_mfma_f32_32x32x16_bf16 v[56:71], v[48:51], v[152:155], v[56:71]\n"
      "v_add_f32 v85, 1.0, v85\n"
      "v_mul_f32 v86, s90, v86\n"
      "v_permlane16_swap_b32 v79, v87\n"
      "v_permlane16_swap_b32 v88, v96\n"
      "v_mfma_f32_32x32x16_bf16 v[56:71], v[52:55], v[156:159], v[56:71]\n"
      "ds_read_b128 v[52:55], v16 offset:45056\n"
      "ds_read_b128 v[48:51], %[xa] offset:45056\n"
      "ds_read_b32 v28, %[fa]\n"
      "v_rcp_f32 v85, v85\n"
      "s_waitcnt lgkmcnt(5)\n"
      "v_mfma_f32_32x32x16_bf16 v[56:71], v[32:35], v[160:163], v[56:71]\n"
      "v_mul_f32 v77, v77, v85\n"
      "v_exp_f32 v86, v86\n"
      "v_mul_f32 v87, s90, v87\n"
      "v_mul_f32 v96, s90, v96\n"
      "v_mfma_f32_32x32x16_bf16 v[56:71], v[36:39], v[164:167], v[56:71]\n"
      "ds_read_b128 v[36:39], v18 offset:45056\n"
      "ds_read_b128 v[32:35], v17 offset:45056\n"
      "v_cvt_pk_bf16_f32 v82, v76, v77\n"
      "v_add_f32 v86, 1.0, v86\n"
      "s_waitcnt lgkmcnt(5)\n"
      "v_mfma_f32_32x32x16_bf16 v[56:71], v[40:43], v[168:171], v[56:71]\n"
      "v_cmp_gt_u32 vcc, 3, v28\n"
      "v_rcp_f32 v86, v86\n"
      "v_exp_f32 v87, v87\n"
      "v_mfma_f32_32x32x16_bf16 v[56:71], v[44:47], v[172:175], v[56:71]\n"
      "s_cbranch_vccnz .LsT1b_%=\n"
      ".LrT1b_%=:\n"
      "ds_read_b128 v[44:47], v16 offset:49152\n"
      "ds_read_b128 v[40:43], %[xa] offset:49152\n"
      "v_mul_f32 v78, v78, v86\n"
      "s_waitcnt lgkmcnt(5)\n"
      "v_mfma_f32_32x32x16_bf16 v[56:71], v[48:51], v[176:179], v[56:71]\n"
      "v_add_f32 v87, 1.0, v87\n"
      "v_exp_f32 v96, v96\n"
      "v_permlane16_swap_b32 v89, v97\n"
      "v_mfma_f32_32x32x16_bf16 v[56:71], v[52:55], v[180:183], v[56:71]\n"
      "ds_read_b128 v[52:55], v18 offset:49152\n"
      "ds_read_b128 v[48:51], v17 offset:49152\n"
      "v_rcp_f32 v87, v87\n"
      "v_add_f32 v96, 1.0, v96\n"
      "s_waitcnt lgkmcnt(4)\n"
      "v_mfma_f32_32x32x16_bf16 v[56:71], v[32:35], v[184:187], v[56:71]\n"
      "v_mul_f32 v79, v79, v87\n"
      "v_rcp_f32 v96, v96\n"
      "v_mul_f32 v97, s90, v97\n"
      "v_permlane16_swap_b32 v90, v98\n"
      "v_mfma_f32_32x32x16_bf16 v[56:71], v[36:39], v[188:191], v[56:71]\n"
      "ds_read_b128 v[36:39], v16 offset:53248\n"
      "ds_read_b128 v[32:35], %[xa] offset:53248\n"
      "v_cvt_pk_bf16_f32 v83, v78, v79\n"
      "ds_write_b128 %[ha], v[80:83] offset:96\n"
      "s_waitcnt lgkmcnt(5)\n"
      "v_mfma_f32_32x32x16_bf16 v[56:71], v[40:43], v[192:195], v[56:71]\n"
      "ds_add_u32 %[sg], v27\n"
      "v_mul_f32 v88, v88, v96\n"
      "v_exp_f32 v97, v97\n"
      "v_mul_f32 v98, s90, v98\n"
      "v_mfma_f32_32x32x16_bf16 v[56:71], v[44:47], v[196:199], v[56:71]\n"
      "ds_read_b128 v[44:47], v18 offset:53248\n"
      "ds_read_b128 v[40:43], v17 offset:53248\n"
      "v_add_f32 v97, 1.0, v97\n"
      "v_exp_f32 v98, v98\n"
      "s_waitcnt lgkmcnt(6)\n"
      "v_mfma_f32_32x32x16_bf16 v[56:71], v[48:51], v[200:203], v[56:71]\n"
      "v_rcp_f32 v97, v97\n"
      "v_add_f32 v98, 1.0, v98\n"
      "v_permlane16_swap_b32 v91, v99\n"
      "v_mfma_f32_32x32x16_bf16 v[56:71], v[52:55], v[204:207], v[56:71]\n"
      "ds_read_b128 v[52:55], v16 offset:57344\n"
      "ds_read_b128 v[48:51], %[xa] offset:57344\n"
      "v_mul_f32 v89, v89, v97\n"
      "v_rcp_f32 v98, v98\n"
      "s_waitcnt lgkmcnt(6)\n"
      "v_mfma_f32_32x32x16_bf16 v[56:71], v[32:35], v[208:211], v[56:71]\n"
      "v_cvt_pk_bf16_f32 v96, v88, v89\n"
      "v_mul_f32 v90, v90, v98\n"
      "v_mul_f32 v99, s90, v99\n"
      "v_permlane16_swap_b32 v92, v100\n"
      "v_mfma_f32_32x32x16_bf16 v[56:71], v[36:39], v[212:215], v[56:71]\n"
      "ds_read_b128 v[36:39], v18 offset:57344\n"
      "ds_read_b128 v[32:35], v17 offset:57344\n"
      "v_exp_f32 v99, v99\n"
      "v_mul_f32 v100, s90, v100\n"
      "s_waitcnt lgkmcnt(4)\n"
      "v_mfma_f32_32x32x16_bf16 v[56:71], v[40:43], v[216:219], v[56:71]\n"
      "v_add_f32 v99, 1.0, v99\n"
      "v_exp_f32 v100, v100\n"
      "v_permlane16_swap_b32 v93, v101\n"
      "v_mfma_f32_32x32x16_bf16 v[56:71], v[44:47], v[220:223], v[56:71]\n"
      "ds_read_b128 v[44:47], v16 offset:61440\n"
      "ds_read_b128 v[40:43], %[xa] offset:61440\n"
      "ds_read_b32 v28, %[fa]\n"
      "v_rcp_f32 v99, v99\n"
      "s_waitcnt lgkmcnt(5)\n"
      "v_mfma_f32_32x32x16_bf16 v[56:71], v[48:51], v[224:227], v[56:71]\n"
      "v_mul_f32 v91, v91, v99\n"
      "v_add_f32 v100, 1.0, v100\n"
      "v_mul_f32 v101, s90, v101\n"
      "v_permlane16_swap_b32 v94, v102\n"
      "v_mfma_f32_32x32x16_bf16 v[56:71], v[52:55], v[228:231], v[56:71]\n"
      "ds_read_b128 v[52:55], v18 offset:61440\n"
      "ds_read_b128 v[48:51], v17 offset:61440\n"
      "v_cvt_pk_bf16_f32 v97, v90, v91\n"
      "v_rcp_f32 v100, v100\n"
      "s_waitcnt lgkmcnt(5)\n"
      "v_mfma_f32_32x32x16_bf16 v[56:71], v[32:35], v[232:235], v[56:71]\n"
      "v_cmp_gt_u32 vcc, 4, v28\n"
      "v_mul_f32 v92, v92, v100\n"
      "v_exp_f32 v101, v101\n"
      "v_mul_f32 v102, s90, v102\n"
      "v_mfma_f32_32x32x16_bf16 v[56:71], v[36:39], v[236:239], v[56:71]\n"
      "s_cbranch_vccnz .LsT2a_%=\n"
      ".LrT2a_%=:\n"
      "ds_read_b128 v[36:39], v20 offset:0\n"
      "ds_read_b128 v[32:35], v19 offset:0\n"
      "v_add_f32 v101, 1.0, v101\n"
      "s_waitcnt lgkmcnt(5)\n"
      "v_mfma_f32_32x32x16_bf16 v[56:71], v[40:43], v[240:243], v[56:71]\n"
      "v_rcp_f32 v101, v101\n"
      "v_exp_f32 v102, v102\n"
      "v_permlane16_swap_b32 v95, v103\n"
      "v_mfma_f32_32x32x16_bf16 v[56:71], v[44:47], v[244:247], v[56:71]\n"
      "ds_read_b128 v[44:47], v22 offset:0\n"
      "ds_read_b128 v[40:43], v21 offset:0\n"
      "v_mul_f32 v93, v93, v101\n"
      "v_add_f32 v102, 1.0, v102\n"
      "s_waitcnt lgkmcnt(4)\n"
      "v_mfma_f32_32x32x16_bf16 v[56:71], v[48:51], v[248:251], v[56:71]\n"
      "v_cvt_pk_bf16_f32 v98, v92, v93\n"
      "v_rcp_f32 v102, v102\n"
      "v_mul_f32 v103, s90, v103\n"
      "v_permlane16_swap_b32 v112, v120\n"
      "v_mfma_f32_32x32x16_bf16 v[56:71], v[52:55], v[252:255], v[56:71]\n"
      "ds_read_b128 v[52:55], v20 offset:4096\n"
      "ds_read_b128 v[48:51], v19 offset:4096\n"
      "v_mul_f32 v94, v94, v102\n"
      "v_exp_f32 v103, v103\n"
      "s_waitcnt lgkmcnt(4)\n"
      "v_mfma_f32_32x32x16_bf16 v[72:87], v[32:35], v[128:131], 0\n"
      "v_add_f32 v103, 1.0, v103\n"
      "v_mul_f32 v120, s90, v120\n"
      "v_permlane16_swap_b32 v113, v121\n"
      "v_permlane16_swap_b32 v114, v122\n"
      "v_mfma_f32_32x32x16_bf16 v[72:87], v[36:39], v[132:135], v[72:87]\n"
      "ds_read_b128 v[36:39], v22 offset:4096\n"
      "ds_read_b128 v[32:35], v21 offset:4096\n"
      "v_rcp_f32 v103, v103\n"
      "v_mul_f32 v121, s90, v121\n"
      "s_waitcnt lgkmcnt(4)\n"
      "v_mfma_f32_32x32x16_bf16 v[72:87], v[40:43], v[136:139], v[72:87]\n"
      "v_mul_f32 v95, v95, v103\n"
      "v_exp_f32 v120, v120\n"
      "v_exp_f32 v121, v121\n"
      "v_mul_f32 v122, s90, v122\n"
      "v_mfma_f32_32x32x16_bf16 v[72:87], v[44:47], v[140:143], v[72:87]\n"
      "ds_read_b128 v[44:47], v20 offset:8192\n"
      "ds_read_b128 v[40:43], v19 offset:8192\n"
      "v_cvt_pk_bf16_f32 v99, v94, v95\n"
      "ds_write_b128 %[ha], v[96:99] offset:160\n"
      "s_waitcnt lgkmcnt(5)\n"
      "v_mfma_f32_32x32x16_bf16 v[72:87], v[48:51], v[144:147], v[72:87]\n"
      "ds_add_u32 %[sg], v27\n"
      "v_add_f32 v120, 1.0, v120\n"
      "v_add_f32 v121, 1.0, v121\n"
      "v_exp_f32 v122, v122\n"
      "v_mfma_f32_32x32x16_bf16 v[72:87], v[52:55], v[148:151], v[72:87]\n"
      "ds_read_b128 v[52:55], v22 offset:8192\n"
      "ds_read_b128 v[48:51], v21 offset:8192\n"
      "v_rcp_f32 v120, v120\n"
      "v_add_f32 v122, 1.0, v122\n"
      "s_waitcnt lgkmcnt(6)\n"
      "v_mfma_f32_32x32x16_bf16 v[72:87], v[32:35], v[152:155], v[72:87]\n"
      "v_mul_f32 v112, v112, v120\n"
      "v_rcp_f32 v121, v121\n"
      "v_rcp_f32 v122, v122\n"
      "v_mfma_f32_32x32x16_bf16 v[72:87], v[36:39], v[156:159], v[72:87]\n"
      "ds_read_b128 v[36:39], v20 offset:12288\n"
      "ds_read_b128 v[32:35], v19 offset:12288\n"
      "ds_read_b32 v28, %[fa]\n"
      "v_mul_f32 v113, v113, v121\n"
      "s_waitcnt lgkmcnt(7)\n"
      "v_mfma_f32_32x32x16_bf16 v[72:87], v[40:43], v[160:163], v[72:87]\n"
      "v_cvt_pk_bf16_f32 v120, v112, v113\n"
      "v_mul_f32 v114, v114, v122\n"
      "v_permlane16_swap_b32 v115, v123\n"
      "v_permlane16_swap_b32 v116, v124\n"
      "v_mfma_f32_32x32x16_bf16 v[72:87], v[44:47], v[164:167], v[72:87]\n"
      "ds_read_b128 v[44:47], v22 offset:12288\n"
      "ds_read_b128 v[40:43], v21 offset:12288\n"
      "v_mul_f32 v123, s90, v123\n"
      "v_mul_f32 v124, s90, v124\n"
      "s_waitcnt lgkmcnt(5)\n"
      "v_mfma_f32_32x32x16_bf16 v[72:87], v[48:51], v[168:171], v[72:87]\n"
      "v_cmp_gt_u32 vcc, 5, v28\n"
      "v_exp_f32 v123, v123\n"
      "v_exp_f32 v124, v124\n"
      "v_mfma_f32_32x32x16_bf16 v[72:87], v[52:55], v[172:175], v[72:87]\n"
      "s_cbranch_vccnz .LsT2b_%=\n"
      ".LrT2b_%=:\n"
      "ds_read_b128 v[52:55], v20 offset:16384\n"
      "ds_read_b128 v[48:51], v19 offset:16384\n"
      "v_add_f32 v123, 1.0, v123\n"
      "s_waitcnt lgkmcnt(5)\n"
      "v_mfma_f32_32x32x16_bf16 v[72:87], v[32:35], v[176:179], v[72:87]\n"
      "v_rcp_f32 v123, v123\n"
      "v_add_f32 v124, 1.0, v124\n"
      "v_permlane16_swap_b32 v117, v125\n"
      "v_mfma_f32_32x32x16_bf16 v[72:87], v[36:39], v[180:183], v[72:87]\n"
      "ds_read_b128 v[36:39], v22 offset:16384\n"
      "ds_read_b128 v[32:35], v21 offset:16384\n"
      "v_mul_f32 v115, v115, v123\n"
      "v_rcp_f32 v124, v124\n"
      "s_waitcnt lgkmcnt(4)\n"
      "v_mfma_f32_32x32x16_bf16 v[72:87], v[40:43], v[184:187], v[72:87]\n"
      "v_cvt_pk_bf16_f32 v121, v114, v115\n"
      "v_mul_f32 v116, v116, v124\n"
      "v_mul_f32 v125, s90, v125\n"
      "v_permlane16_swap_b32 v118, v126\n"
      "v_mfma_f32_32x32x16_bf16 v[72:87], v[44:47], v[188:191], v[72:87]\n"
      "ds_read_b128 v[44:47], v20 offset:20480\n"
      "ds_read_b128 v[40:43], v19 offset:20480\n"
      "v_exp_f32 v125, v125\n"
      "v_mul_f32 v126, s90, v126\n"
      "s_waitcnt lgkmcnt(4)\n"
      "v_mfma_f32_32x32x16_bf16 v[72:87], v[48:51], v[192:195], v[72:87]\n"
      "v_add_f32 v125, 1.0, v125\n"
      "v_exp_f32 v126, v126\n"
      "v_permlane16_swap_b32 v119, v127\n"
      "v_mfma_f32_32x32x16_bf16 v[72:87], v[52:55], v[196:199], v[72:87]\n"
      "ds_read_b128 v[52:55], v22 offset:20480\n"
      "ds_read_b128 v[48:51], v21 offset:20480\n"
      "v_rcp_f32 v125, v125\n"
      "v_add_f32 v126, 1.0, v126\n"
      "s_waitcnt lgkmcnt(4)\n"
      "v_mfma_f32_32x32x16_bf16 v[72:87], v[32:35], v[200:203], v[72:87]\n"
      "v_mul_f32 v117, v117, v125\n"
      "v_rcp_f32 v126, v126\n"
      "v_mul_f32 v127, s90, v127\n"
      "v_permlane16_swap_b32 v56, v64\n"
      "v_mfma_f32_32x32x16_bf16 v[72:87], v[36:39], v[204:207], v[72:87]\n"
      "ds_read_b128 v[36:39], v20 offset:24576\n"
      "ds_read_b128 v[32:35], v19 offset:24576\n"
      "v_cvt_pk_bf16_f32 v122, v116, v117\n"
      "v_mul_f32 v118, v118, v126\n"
      "s_waitcnt lgkmcnt(4)\n"
      "v_mfma_f32_32x32x16_bf16 v[72:87], v[40:43], v[208:211], v[72:87]\n"
      "v_exp_f32 v127, v127\n"
      "v_mul_f32 v64, s90, v64\n"
      "v_permlane16_swap_b32 v57, v65\n"
      "v_mfma_f32_32x32x16_bf16 v[72:87], v[44:47], v[212:215], v[72:87]\n"
      "ds_read_b128 v[44:47], v22 offset:24576\n"
      "ds_read_b128 v[40:43], v21 offset:24576\n"
      "v_add_f32 v127, 1.0, v127\n"
      "v_exp_f32 v64, v64\n"
      "s_waitcnt lgkmcnt(4)\n"
      "v_mfma_f32_32x32x16_bf16 v[72:87], v[48:51], v[216:219], v[72:87]\n"
      "v_rcp_f32 v127, v127\n"
      "v_add_f32 v64, 1.0, v64\n"
      "v_mul_f32 v65, s90, v65\n"
      "v_permlane16_swap_b32 v58, v66\n"
      "v_mfma_f32_32x32x16_bf16 v[72:87], v[52:55], v[220:223], v[72:87]\n"
      "ds_read_b128 v[52:55], v20 offset:28672\n"
      "ds_read_b128 v[48:51], v19 offset:28672\n"
      "ds_read_b32 v28, %[fa]\n"
      "v_mul_f32 v119, v119, v127\n"
      "s_waitcnt lgkmcnt(5)\n"
      "v_mfma_f32_32x32x16_bf16 v[72:87], v[32:35], v[224:227], v[72:87]\n"
      "v_cvt_pk_bf16_f32 v123, v118, v119\n"
      "ds_write_b128 %[ha], v[120:123] offset:224\n"
      "ds_add_u32 %[sg], v27\n"
      "v_rcp_f32 v64, v64\n"
      "v_mul_f32 v66, s90, v66\n"
      "v_mfma_f32_32x32x16_bf16 v[72:87], v[36:39], v[228:231], v[72:87]\n"
      "ds_read_b128 v[36:39], v22 offset:28672\n"
      "ds_read_b128 v[32:35], v21 offset:28672\n"
      "v_mul_f32 v56, v56, v64\n"
      "v_exp_f32 v65, v65\n"
      "s_waitcnt lgkmcnt(7)\n"
      "v_mfma_f32_32x32x16_bf16 v[72:87], v[40:43], v[232:235], v[72:87]\n"
      "v_cmp_gt_u32 vcc, 6, v28\n"
      "v_add_f32 v65, 1.0, v65\n"
      "v_exp_f32 v66, v66\n"
      "v_permlane16_swap_b32 v59, v67\n"
      "v_mfma_f32_32x32x16_bf16 v[72:87], v[44:47], v[236:239], v[72:87]\n"
      "s_cbranch_vccnz .LsT3a_%=\n"
      ".LrT3a_%=:\n"
      "ds_read_b128 v[44:47], v20 offset:32768\n"
      "ds_read_b128 v[40:43], v19 offset:32768\n"
      "v_rcp_f32 v65, v65\n"
      "s_waitcnt lgkmcnt(7)\n"
      "v_mfma_f32_32x32x16_bf16 v[72:87], v[48:51], v[240:243], v[72:87]\n"
      "v_mul_f32 v57, v57, v65\n"
      "v_add_f32 v66, 1.0, v66\n"
      "v_mul_f32 v67, s90, v67\n"
      "v_permlane16_swap_b32 v60, v68\n"
      "v_mfma_f32_32x32x16_bf16 v[72:87], v[52:55], v[244:247], v[72:87]\n"
      "ds_read_b128 v[52:55], v22 offset:32768\n"
      "ds_read_b128 v[48:51], v21 offset:32768\n"
      "v_cvt_pk_bf16_f32 v64, v56, v57\n"
      "v_rcp_f32 v66, v66\n"
      "s_waitcnt lgkmcnt(4)\n"
      "v_mfma_f32_32x32x16_bf16 v[72:87], v[32:35], v[248:251], v[72:87]\n"
      "v_mul_f32 v58, v58, v66\n"
      "v_exp_f32 v67, v67\n"
      "v_mul_f32 v68, s90, v68\n"
      "v_permlane16_swap_b32 v61, v69\n"
      "v_mfma_f32_32x32x16_bf16 v[72:87], v[36:39], v[252:255], v[72:87]\n"
      "ds_read_b128 v[36:39], v20 offset:36864\n"
      "ds_read_b128 v[32:35], v19 offset:36864\n"
      "v_add_f32 v67, 1.0, v67\n"
      "v_exp_f32 v68, v68\n"
      "s_waitcnt lgkmcnt(4)\n"
      "v_mfma_f32_32x32x16_bf16 v[88:103], v[40:43], v[128:131], 0\n"
      "v_rcp_f32 v67, v67\n"
      "v_add_f32 v68, 1.0, v68\n"
      "v_mul_f32 v69, s90, v69\n"
      "v_permlane16_swap_b32 v62, v70\n"
      "v_mfma_f32_32x32x16_bf16 v[88:103], v[44:47], v[132:135], v[88:103]\n"
      "ds_read_b128 v[44:47], v22 offset:36864\n"
      "ds_read_b128 v[40:43], v21 offset:36864\n"
      "v_mul_f32 v59, v59, v67\n"
      "v_rcp_f32 v68, v68\n"
      "s_waitcnt lgkmcnt(4)\n"
      "v_mfma_f32_32x32x16_bf16 v[88:103], v[48:51], v[136:139], v[88:103]\n"
      "v_cvt_pk_bf16_f32 v65, v58, v59\n"
      "v_mul_f32 v60, v60, v68\n"
      "v_exp_f32 v69, v69\n"
      "v_mul_f32 v70, s90, v70\n"
      "v_mfma_f32_32x32x16_bf16 v[88:103], v[52:55], v[140:143], v[88:103]\n"
      "ds_read_b128 v[52:55], v20 offset:40960\n"
      "ds_read_b128 v[48:51], v19 offset:40960\n"
      "v_add_f32 v69, 1.0, v69\n"
      "v_exp_f32 v70, v70\n"
      "s_waitcnt lgkmcnt(4)\n"
      "v_mfma_f32_32x32x16_bf16 v[88:103], v[32:35], v[144:147], v[88:103]\n"
      "v_rcp_f32 v69, v69\n"
      "v_add_f32 v70, 1.0, v70\n"
      "v_permlane16_swap_b32 v63, v71\n"
      "v_mfma_f32_32x32x16_bf16 v[88:103], v[36:39], v[148:151], v[88:103]\n"
      "ds_read_b128 v[36:39], v22 offset:40960\n"
      "ds_read_b128 v[32:35], v21 offset:40960\n"
      "v_mul_f32 v61, v61, v69\n"
      "v_rcp_f32 v70, v70\n"
      "s_waitcnt lgkmcnt(4)\n"
      "v_mfma_f32_32x32x16_bf16 v[88:103], v[40:43], v[152:155], v[88:103]\n"
      "v_cvt_pk_bf16_f32 v66, v60, v61\n"
      "v_mul_f32 v62, v62, v70\n"
      "v_mul_f32 v71, s90, v71\n"
      "v_permlane16_swap_b32 v72, v80\n"
      "v_mfma_f32_32x32x16_bf16 v[88:103], v[44:47], v[156:159], v[88:103]\n"
      "ds_read_b128 v[44:47], v20 offset:45056\n"
      "ds_read_b128 v[40:43], v19 offset:45056\n"
      "ds_read_b32 v28, %[fa]\n"
      "v_exp_f32 v71, v71\n"
      "s_waitcnt lgkmcnt(5)\n"
      "v_mfma_f32_32x32x16_bf16 v[88:103], v[48:51], v[160:163], v[88:103]\n"
      "v_add_f32 v71, 1.0, v71\n"
      "v_mul_f32 v80, s90, v80\n"
      "v_permlane16_swap_b32 v73, v81\n"
      "v_permlane16_swap_b32 v74, v82\n"
      "v_mfma_f32_32x32x16_bf16 v[88:103], v[52:55], v[164:167], v[88:103]\n"
      "ds_read_b128 v[52:55], v22 offset:45056\n"
      "ds_read_b128 v[48:51], v21 offset:45056\n"
      "v_rcp_f32 v71, v71\n"
      "v_mul_f32 v81, s90, v81\n"
      "s_waitcnt lgkmcnt(5)\n"
      "v_mfma_f32_32x32x16_bf16 v[88:103], v[32:35], v[168:171], v[88:103]\n"
      "v_cmp_gt_u32 vcc, 7, v28\n"
      "v_mul_f32 v63, v63, v71\n"
      "v_exp_f32 v80, v80\n"
      "v_exp_f32 v81, v81\n"
      "v_mfma_f32_32x32x16_bf16 v[88:103], v[36:39], v[172:175], v[88:103]\n"
      "s_cbranch_vccnz .LsT3b_%=\n"
      ".LrT3b_%=:\n"
      "ds_read_b128 v[36:39], v20 offset:49152\n"
      "ds_read_b128 v[32:35], v19 offset:49152\n"
      "v_cvt_pk_bf16_f32 v67, v62, v63\n"
      "s_waitcnt lgkmcnt(5)\n"
      "v_mfma_f32_32x32x16_bf16 v[88:103], v[40:43], v[176:179], v[88:103]\n"
      "ds_write_b128 %[ha], v[64:67] offset:288\n"
      "ds_add_u32 %[sg], v27\n"
      "v_add_f32 v80, 1.0, v80\n"
      "v_add_f32 v81, 1.0, v81\n"
      "v_mul_f32 v82, s90, v82\n"
      "v_mfma_f32_32x32x16_bf16 v[88:103], v[44:47], v[180:183], v[88:103]\n"
      "ds_read_b128 v[44:47], v22 offset:49152\n"
      "ds_read_b128 v[40:43], v21 offset:49152\n"
      "v_rcp_f32 v80, v80\n"
      "s_waitcnt lgkmcnt(6)\n"
      "v_mfma_f32_32x32x16_bf16 v[88:103], v[48:51], v[184:187], v[88:103]\n"
      "v_mul_f32 v72, v72, v80\n"
      "v_rcp_f32 v81, v81\n"
      "v_exp_f32 v82, v82\n"
      "v_mfma_f32_32x32x16_bf16 v[88:103], v[52:55], v[188:191], v[88:103]\n"
      "ds_read_b128 v[52:55], v20 offset:53248\n"
      "ds_read_b128 v[48:51], v19 offset:53248\n"
      "v_mul_f32 v73, v73, v81\n"
      "v_add_f32 v82, 1.0, v82\n"
      "s_waitcnt lgkmcnt(6)\n"
      "v_mfma_f32_32x32x16_bf16 v[88:103], v[32:35], v[192:195], v[88:103]\n"
      "v_cvt_pk_bf16_f32 v80, v72, v73\n"
      "v_rcp_f32 v82, v82\n"
      "v_permlane16_swap_b32 v75, v83\n"
      "v_mfma_f32_32x32x16_bf16 v[88:103], v[36:39], v[196:199], v[88:103]\n"
      "ds_read_b128 v[36:39], v22 offset:53248\n"
      "ds_read_b128 v[32:35], v21 offset:53248\n"
      "v_mul_f32 v74, v74, v82\n"
      "v_mul_f32 v83, s90, v83\n"
      "s_waitcnt lgkmcnt(4)\n"
      "v_mfma_f32_32x32x16_bf16 v[88:103], v[40:43], v[200:203], v[88:103]\n"
      "v_exp_f32 v83, v83\n"
      "v_permlane16_swap_b32 v76, v84\n"
      "v_permlane16_swap_b32 v77, v85\n"
      "v_mfma_f32_32x32x16_bf16 v[88:103], v[44:47], v[204:207], v[88:103]\n"
      "ds_read_b128 v[44:47], v20 offset:57344\n"
      "ds_read_b128 v[40:43], v19 offset:57344\n"
      "v_add_f32 v83, 1.0, v83\n"
      "v_mul_f32 v84, s90, v84\n"
      "s_waitcnt lgkmcnt(4)\n"
      "v_mfma_f32_32x32x16_bf16 v[88:103], v[48:51], v[208:211], v[88:103]\n"
      "v_rcp_f32 v83, v83\n"
      "v_exp_f32 v84, v84\n"
      "v_mul_f32 v85, s90, v85\n"
      "v_mfma_f32_32x32x16_bf16 v[88:103], v[52:55], v[212:215], v[88:103]\n"
      "ds_read_b128 v[52:55], v22 offset:57344\n"
      "ds_read_b128 v[48:51], v21 offset:57344\n"
      "v_mul_f32 v75, v75, v83\n"
      "v_add_f32 v84, 1.0, v84\n"
      "s_waitcnt lgkmcnt(4)\n"
      "v_mfma_f32_32x32x16_bf16 v[88:103], v[32:35], v[216:219], v[88:103]\n"
      "v_cvt_pk_bf16_f32 v81, v74, v75\n"
      "v_rcp_f32 v84, v84\n"
      "v_exp_f32 v85, v85\n"
      "v_mfma_f32_32x32x16_bf16 v[88:103], v[36:39], v[220:223], v[88:103]\n"
      "ds_read_b128 v[36:39], v20 offset:61440\n"
      "ds_read_b128 v[32:35], v19 offset:61440\n"
      "ds_read_b32 v28, %[fa]\n"
      "v_mul_f32 v76, v76, v84\n"
      "s_waitcnt lgkmcnt(5)\n"
      "v_mfma_f32_32x32x16_bf16 v[88:103], v[40:43], v[224:227], v[88:103]\n"
      "v_add_f32 v85, 1.0, v85\n"
      "v_permlane16_swap_b32 v78, v86\n"
      "v_permlane16_swap_b32 v79, v87\n"
      "v_mfma_f32_32x32x16_bf16 v[88:103], v[44:47], v[228:231], v[88:103]\n"
      "ds_read_b128 v[44:47], v22 offset:61440\n"
      "ds_read_b128 v[40:43], v21 offset:61440\n"
      "v_rcp_f32 v85, v85\n"
      "v_mul_f32 v86, s90, v86\n"
      "s_waitcnt lgkmcnt(5)\n"
      "v_mfma_f32_32x32x16_bf16 v[88:103], v[48:51], v[232:235], v[88:103]\n"
      "v_cmp_gt_u32 vcc, 7, v28\n"
      "v_mul_f32 v77, v77, v85\n"
      "v_exp_f32 v86, v86\n"
      "v_mul_f32 v87, s90, v87\n"
      "v_mfma_f32_32x32x16_bf16 v[88:103], v[52:55], v[236:239], v[88:103]\n"
      "s_cbranch_vccnz .LsT4a_%=\n"
      ".LrT4a_%=:\n"
      "ds_read_b128 v[52:55], v16 offset:0\n"
      "ds_read_b128 v[48:51], %[xa] offset:0\n"
      "v_cvt_pk_bf16_f32 v82, v76, v77\n"
      "s_waitcnt lgkmcnt(5)\n"
      "v_mfma_f32_32x32x16_bf16 v[88:103], v[32:35], v[240:243], v[88:103]\n"
      "v_add_f32 v86, 1.0, v86\n"
      "v_exp_f32 v87, v87\n"
      "v_mfma_f32_32x32x16_bf16 v[88:103], v[36:39], v[244:247], v[88:103]\n"
      "ds_read_b128 v[36:39], v18 offset:0\n"
      "ds_read_b128 v[32:35], v17 offset:0\n"
      "v_rcp_f32 v86, v86\n"
      "v_add_f32 v87, 1.0, v87\n"
      "s_waitcnt lgkmcnt(4)\n"
      "v_mfma_f32_32x32x16_bf16 v[88:103], v[40:43], v[248:251], v[88:103]\n"
      "ds_read_b32 v28, %[fa]\n"
      "v_mul_f32 v78, v78, v86\n"
      "v_rcp_f32 v87, v87\n"
      "v_mfma_f32_32x32x16_bf16 v[88:103], v[44:47], v[252:255], v[88:103]\n"
      "ds_read_b128 v[44:47], v16 offset:4096\n"
      "ds_read_b128 v[40:43], %[xa] offset:4096\n"
      "v_mul_f32 v79, v79, v87\n"
      "s_waitcnt lgkmcnt(5)\n"
      "v_mfma_f32_32x32x16_bf16 v[112:127], v[48:51], v[128:131], 0\n"
      "v_cvt_pk_bf16_f32 v83, v78, v79\n"
      "v_mfma_f32_32x32x16_bf16 v[112:127], v[52:55], v[132:135], v[112:127]\n"
      "ds_read_b128 v[52:55], v18 offset:4096\n"
      "ds_read_b128 v[48:51], v17 offset:4096\n"
      "v_cmp_gt_u32 vcc, 9, v28\n"
      "s_waitcnt lgkmcnt(5)\n"
      "v_mfma_f32_32x32x16_bf16 v[112:127], v[32:35], v[136:139], v[112:127]\n"
      "s_cbranch_vccnz .LsV0_%=\n"
      ".LrV0_%=:\n"
      "ds_read_b64_tr_b16 v[128:129], %[wv] offset:32768\n"
      "ds_read_b64_tr_b16 v[130:131], %[wv] offset:33792\n"
      "ds_read_b32 v28, %[fa]\n"
      "v_mfma_f32_32x32x16_bf16 v[112:127], v[36:39], v[140:143], v[112:127]\n"
      "ds_read_b64_tr_b16 v[132:133], %[wv] offset:36864\n"
      "ds_read_b64_tr_b16 v[134:135], %[wv] offset:37888\n"
      "ds_read_b128 v[36:39], v16 offset:8192\n"
      "ds_read_b128 v[32:35], %[xa] offset:8192\n"
      "v_permlane16_swap_b32 v88, v96\n"
      "s_waitcnt lgkmcnt(9)\n"
      "v_mfma_f32_32x32x16_bf16 v[112:127], v[40:43], v[144:147], v[112:127]\n"
      "ds_read_b64_tr_b16 v[136:137], %[wv] offset:40960\n"
      "ds_read_b64_tr_b16 v[138:139], %[wv] offset:41984\n"
      "v_mul_f32 v96, s90, v96\n"
      "v_permlane16_swap_b32 v89, v97\n"
      "v_permlane16_swap_b32 v90, v98\n"
      "v_mfma_f32_32x32x16_bf16 v[112:127], v[44:47], v[148:151], v[112:127]\n"
      "ds_read_b64_tr_b16 v[140:141], %[wv] offset:45056\n"
      "ds_read_b64_tr_b16 v[142:143], %[wv] offset:46080\n"
      "ds_read_b128 v[44:47], v18 offset:8192\n"
      "ds_read_b128 v[40:43], v17 offset:8192\n"
      "v_cmp_gt_u32 vcc, 10, v28\n"
      "v_exp_f32 v96, v96\n"
      "s_waitcnt lgkmcnt(13)\n"
      "v_mfma_f32_32x32x16_bf16 v[112:127], v[48:51], v[152:155], v[112:127]\n"
      "s_cbranch_vccnz .LsV1_%=\n"
      ".LrV1_%=:\n"
      "ds_read_b64_tr_b16 v[144:145], %[wv] offset:49152\n"
      "ds_read_b64_tr_b16 v[146:147], %[wv] offset:50176\n"
      "ds_read_b32 v28, %[fa]\n"
      "v_add_f32 v96, 1.0, v96\n"
      "v_mul_f32 v97, s90, v97\n"
      "v_mul_f32 v98, s90, v98\n"
      "v_mfma_f32_32x32x16_bf16 v[112:127], v[52:55], v[156:159], v[112:127]\n"
      "ds_read_b64_tr_b16 v[148:149], %[wv] offset:53248\n"
      "ds_read_b64_tr_b16 v[150:151], %[wv] offset:54272\n"
      "ds_read_b128 v[52:55], v16 offset:12288\n"
      "ds_read_b128 v[48:51], %[xa] offset:12288\n"
      "ds_read_b32 v28, %[fa]\n"
      "v_rcp_f32 v96, v96\n"
      "s_waitcnt lgkmcnt(14)\n"
      "v_mfma_f32_32x32x16_bf16 v[112:127], v[32:35], v[160:163], v[112:127]\n"
      "ds_read_b64_tr_b16 v[152:153], %[wv] offset:57344\n"
      "ds_read_b64_tr_b16 v[154:155], %[wv] offset:58368\n"
      "v_mul_f32 v88, v88, v96\n"
      "v_exp_f32 v97, v97\n"
      "v_exp_f32 v98, v98\n"
      "v_mfma_f32_32x32x16_bf16 v[112:127], v[36:39], v[164:167], v[112:127]\n"
      "ds_read_b64_tr_b16 v[156:157], %[wv] offset:61440\n"
      "ds_read_b64_tr_b16 v[158:159], %[wv] offset:62464\n"
      "ds_read_b128 v[36:39], v18 offset:12288\n"
      "ds_read_b128 v[32:35], v17 offset:12288\n"
      "v_cmp_gt_u32 vcc, 11, v28\n"
      "v_add_f32 v97, 1.0, v97\n"
      "s_waitcnt lgkmcnt(14)\n"
      "v_mfma_f32_32x32x16_bf16 v[112:127], v[40:43], v[168:171], v[112:127]\n"
      "s_cbranch_vccnz .LsV2_%=\n"
      ".LrV2_%=:\n"
      "ds_read_b64_tr_b16 v[160:161], v24 offset:0\n"
      "ds_read_b64_tr_b16 v[162:163], v24 offset:1024\n"
      "v_cmp_gt_u32 vcc, 8, v28\n"
      "ds_read_b32 v28, %[fa]\n"
      "ds_write_b128 %[ha], v[80:83] offset:352\n"
      "ds_add_u32 %[sg], v27\n"
      "v_mfma_f32_32x32x16_bf16 v[112:127], v[44:47], v[172:175], v[112:127]\n"
      "s_cbranch_vccnz .LsT4b_%=\n"
      ".LrT4b_%=:\n"
      "ds_read_b64_tr_b16 v[164:165], v24 offset:4096\n"
      "ds_read_b64_tr_b16 v[166:167], v24 offset:5120\n"
      "ds_read_b128 v[44:47], v16 offset:16384\n"
      "ds_read_b128 v[40:43], %[xa] offset:16384\n"
      "v_rcp_f32 v97, v97\n"
      "s_waitcnt lgkmcnt(15)\n"
      "v_mfma_f32_32x32x16_bf16 v[112:127], v[48:51], v[176:179], v[112:127]\n"
      "ds_read_b64_tr_b16 v[168:169], v24 offset:8192\n"
      "ds_read_b64_tr_b16 v[170:171], v24 offset:9216\n"
      "v_mul_f32 v89, v89, v97\n"
      "v_add_f32 v98, 1.0, v98\n"
      "v_permlane16_swap_b32 v91, v99\n"
      "v_permlane16_swap_b32 v92, v100\n"
      "v_mfma_f32_32x32x16_bf16 v[112:127], v[52:55], v[180:183], v[112:127]\n"
      "ds_read_b64_tr_b16 v[172:173], v24 offset:12288\n"
      "ds_read_b64_tr_b16 v[174:175], v24 offset:13312\n"
      "ds_read_b128 v[52:55], v18 offset:16384\n"
      "ds_read_b128 v[48:51], v17 offset:16384\n"
      "v_cmp_gt_u32 vcc, 12, v28\n"
      "v_cvt_pk_bf16_f32 v96, v88, v89\n"
      "s_waitcnt lgkmcnt(15)\n"
      "v_mfma_f32_32x32x16_bf16 v[112:127], v[32:35], v[184:187], v[112:127]\n"
      "s_cbranch_vccnz .LsV3_%=\n"
      ".LrV3_%=:\n"
      "ds_read_b64_tr_b16 v[176:177], v24 offset:16384\n"
      "ds_read_b64_tr_b16 v[178:179], v24 offset:17408\n"
      "ds_read_b32 v28, %[fa]\n"
      "v_rcp_f32 v98, v98\n"
      "v_mul_f32 v99, s90, v99\n"
      "v_mul_f32 v100, s90, v100\n"
      "v_mfma_f32_32x32x16_bf16 v[112:127], v[36:39], v[188:191], v[112:127]\n"
      "ds_read_b64_tr_b16 v[180:181], v24 offset:20480\n"
      "ds_read_b64_tr_b16 v[182:183], v24 offset:21504\n"
      "ds_read_b128 v[36:39], v16 offset:20480\n"
      "ds_read_b128 v[32:35], %[xa] offset:20480\n"
      "v_mul_f32 v90, v90, v98\n"
      "v_exp_f32 v99, v99\n"
      "s_waitcnt lgkmcnt(13)\n"
      "v_mfma_f32_32x32x16_bf16 v[112:127], v[40:43], v[192:195], v[112:127]\n"
      "ds_read_b64_tr_b16 v[184:185], v24 offset:24576\n"
      "ds_read_b64_tr_b16 v[186:187], v24 offset:25600\n"
      "v_add_f32 v99, 1.0, v99\n"
      "v_exp_f32 v100, v100\n"
      "v_permlane16_swap_b32 v93, v101\n"
      "v_mfma_f32_32x32x16_bf16 v[112:127], v[44:47], v[196:199], v[112:127]\n"
      "ds_read_b64_tr_b16 v[188:189], v24 offset:28672\n"
      "ds_read_b64_tr_b16 v[190:191], v24 offset:29696\n"
      "ds_read_b128 v[44:47], v18 offset:20480\n"
      "ds_read_b128 v[40:43], v17 offset:20480\n"
      "v_cmp_gt_u32 vcc, 12, v28\n"
      "v_rcp_f32 v99, v99\n"
      "s_waitcnt lgkmcnt(13)\n"
      "v_mfma_f32_32x32x16_bf16 v[112:127], v[48:51], v[200:203], v[112:127]\n"
      "s_cbranch_vccnz .LsV4_%=\n"
      ".LrV4_%=:\n"
      "ds_read_b64_tr_b16 v[192:193], v24 offset:32768\n"
      "ds_read_b64_tr_b16 v[194:195], v24 offset:33792\n"
      "v_mul_f32 v91, v91, v99\n"
      "v_add_f32 v100, 1.0, v100\n"
      "v_mul_f32 v101, s90, v101\n"
      "v_permlane16_swap_b32 v94, v102\n"
      "v_mfma_f32_32x32x16_bf16 v[112:127], v[52:55], v[204:207], v[112:127]\n"
      "ds_read_b64_tr_b16 v[196:197], v24 offset:36864\n"
      "ds_read_b64_tr_b16 v[198:199], v24 offset:37888\n"
      "ds_read_b128 v[52:55], v16 offset:24576\n"
      "ds_read_b128 v[48:51], %[xa] offset:24576\n"
      "v_cvt_pk_bf16_f32 v97, v90, v91\n"
      "v_rcp_f32 v100, v100\n"
      "s_waitcnt lgkmcnt(12)\n"
      "v_mfma_f32_32x32x16_bf16 v[112:127], v[32:35], v[208:211], v[112:127]\n"
      "ds_read_b64_tr_b16 v[200:201], v24 offset:40960\n"
      "ds_read_b64_tr_b16 v[202:203], v24 offset:41984\n"
      "v_mul_f32 v92, v92, v100\n"
      "v_exp_f32 v101, v101\n"
      "v_mul_f32 v102, s90, v102\n"
      "v_permlane16_swap_b32 v95, v103\n"
      "v_mfma_f32_32x32x16_bf16 v[112:127], v[36:39], v[212:215], v[112:127]\n"
      "ds_read_b64_tr_b16 v[204:205], v24 offset:45056\n"
      "ds_read_b64_tr_b16 v[206:207], v24 offset:46080\n"
      "ds_read_b128 v[36:39], v18 offset:24576\n"
      "ds_read_b128 v[32:35], v17 offset:24576\n"
      "v_add_f32 v101, 1.0, v101\n"
      "v_exp_f32 v102, v102\n"
      "s_waitcnt lgkmcnt(12)\n"
      "v_mfma_f32_32x32x16_bf16 v[112:127], v[40:43], v[216:219], v[112:127]\n"
      "v_rcp_f32 v101, v101\n"
      "v_add_f32 v102, 1.0, v102\n"
      "v_mul_f32 v103, s90, v103\n"
      "v_mfma_f32_32x32x16_bf16 v[112:127], v[44:47], v[220:223], v[112:127]\n"
      "ds_read_b128 v[44:47], v16 offset:28672\n"
      "ds_read_b128 v[40:43], %[xa] offset:28672\n"
      "v_mul_f32 v93, v93, v101\n"
      "v_rcp_f32 v102, v102\n"
      "v_exp_f32 v103, v103\n"
      "s_waitcnt lgkmcnt(8)\n"
      "v_mfma_f32_32x32x16_bf16 v[112:127], v[48:51], v[224:227], v[112:127]\n"
      "v_cvt_pk_bf16_f32 v98, v92, v93\n"
      "v_mul_f32 v94, v94, v102\n"
      "v_add_f32 v103, 1.0, v103\n"
      "v_mfma_f32_32x32x16_bf16 v[112:127], v[52:55], v[228:231], v[112:127]\n"
      "ds_read_b128 v[52:55], v18 offset:28672\n"
      "ds_read_b128 v[48:51], v17 offset:28672\n"
      "v_rcp_f32 v103, v103\n"
      "s_waitcnt lgkmcnt(4)\n"
      "v_mfma_f32_32x32x16_bf16 v[112:127], v[32:35], v[232:235], v[112:127]\n"
      "v_mul_f32 v95, v95, v103\n"
      "v_mfma_f32_32x32x16_bf16 v[112:127], v[36:39], v[236:239], v[112:127]\n"
      "v_cvt_pk_bf16_f32 v99, v94, v95\n"
      "ds_write_b128 %[ha], v[96:99] offset:0\n"
      "ds_add_u32 %[sg], v27\n"
      "s_waitcnt lgkmcnt(4)\n"
      "v_mfma_f32_32x32x16_bf16 v[112:127], v[40:43], v[240:243], v[112:127]\n"
      "v_mfma_f32_32x32x16_bf16 v[112:127], v[44:47], v[244:247], v[112:127]\n"
      "s_waitcnt lgkmcnt(2)\n"
      "v_mfma_f32_32x32x16_bf16 v[112:127], v[48:51], v[248:251], v[112:127]\n"
      "v_mfma_f32_32x32x16_bf16 v[112:127], v[52:55], v[252:255], v[112:127]\n"
      "ds_read_b32 v28, %[fa]\n"
      "s_nop 7\n"
      "s_nop 3\n"
      "v_permlane16_swap_b32 v112, v120\n"
      "v_permlane16_swap_b32 v113, v121\n"
      "v_permlane16_swap_b32 v114, v122\n"
      "v_permlane16_swap_b32 v115, v123\n"
      "v_permlane16_swap_b32 v116, v124\n"
      "v_permlane16_swap_b32 v117, v125\n"
      "v_permlane16_swap_b32 v118, v126\n"
      "v_permlane16_swap_b32 v119, v127\n"
      "v_mul_f32 v120, s90, v120\n"
      "v_mul_f32 v121, s90, v121\n"
      "v_mul_f32 v122, s90, v122\n"
      "v_mul_f32 v123, s90, v123\n"
      "v_mul_f32 v124, s90, v124\n"
      "v_mul_f32 v125, s90, v125\n"
      "v_mul_f32 v126, s90, v126\n"
      "v_mul_f32 v127, s90, v127\n"
      "v_exp_f32 v120, v120\n"
      "v_exp_f32 v121, v121\n"
      "v_exp_f32 v122, v122\n"
      "v_exp_f32 v123, v123\n"
      "v_exp_f32 v124, v124\n"
      "v_exp_f32 v125, v125\n"
      "v_exp_f32 v126, v126\n"
      "v_exp_f32 v127, v127\n"
      "v_cmp_gt_u32 vcc, 13, v28\n"
      "v_add_f32 v120, 1.0, v120\n"
      "v_add_f32 v121, 1.0, v121\n"
      "v_add_f32 v122, 1.0, v122\n"
      "v_add_f32 v123, 1.0, v123\n"
      "v_add_f32 v124, 1.0, v124\n"
      "v_add_f32 v125, 1.0, v125\n"
      "v_add_f32 v126, 1.0, v126\n"
      "v_add_f32 v127, 1.0, v127\n"
      "s_cbranch_vccz .LrS0a_%=\n"
      ".LsS0a_%=:\n"
      "ds_read_b32 v28, %[fa]\n"
      "s_addk_i32 %[fuel], -1\n"
      "s_cmp_lt_i32 %[fuel], 0\n"
      "s_waitcnt lgkmcnt(0)\n"
      "s_cbranch_scc1 .LrS0a_%=\n"
      "v_cmp_gt_u32 vcc, 13, v28\n"
      "s_cbranch_vccnz .LsS0a_%=\n"
      ".LrS0a_%=:\n"
      "ds_read_b64_tr_b16 v[40:41], v25 offset:49152\n"
      "ds_read_b64_tr_b16 v[42:43], v25 offset:49408\n"
      "v_rcp_f32 v120, v120\n"
      "v_rcp_f32 v121, v121\n"
      "v_rcp_f32 v122, v122\n"
      "v_rcp_f32 v123, v123\n"
      "v_rcp_f32 v124, v124\n"
      "v_rcp_f32 v125, v125\n"
      "v_rcp_f32 v126, v126\n"
      "ds_read_b64_tr_b16 v[44:45], v25 offset:50176\n"
      "ds_read_b64_tr_b16 v[46:47], v25 offset:50432\n"
      "v_mul_f32 v112, v112, v120\n"
      "v_mul_f32 v113, v113, v121\n"
      "v_mul_f32 v114, v114, v122\n"
      "v_mul_f32 v115, v115, v123\n"
      "v_mul_f32 v116, v116, v124\n"
      "v_mul_f32 v117, v117, v125\n"
      "v_mul_f32 v118, v118, v126\n"
      "v_rcp_f32 v127, v127\n"
      "ds_read_b64_tr_b16 v[48:49], v25 offset:51200\n"
      "ds_read_b64_tr_b16 v[50:51], v25 offset:51456\n"
      "v_cvt_pk_bf16_f32 v120, v112, v113\n"
      "v_cvt_pk_bf16_f32 v121, v114, v115\n"
      "v_cvt_pk_bf16_f32 v122, v116, v117\n"
      "v_mul_f32 v119, v119, v127\n"
      "ds_read_b64_tr_b16 v[52:53], v25 offset:52224\n"
      "ds_read_b64_tr_b16 v[54:55], v25 offset:52480\n"
      "v_cvt_pk_bf16_f32 v123, v118, v119\n"
      "s_mov_b64 exec, %[ml]\n"
      "v_and_b32 v120, 0xffff, v120\n"
      "v_mov_b32 v121, 0\n"
      "v_mov_b32 v122, 0\n"
      "v_mov_b32 v123, 0\n"
      "s_mov_b32 exec_lo, 0\n"
      "v_mov_b32 v120, 0\n"
      "s_mov_b64 exec, -1\n"
      "ds_write_b128 %[ha], v[120:123] offset:64\n"
      "ds_add_u32 %[sg], v27\n"
      "s_waitcnt lgkmcnt(8)\n"
      "v_mfma_f32_32x32x16_bf16 v[56:71], v[128:131], v[40:43], 0\n"
      "ds_read_b64_tr_b16 v[32:33], v25 offset:53248\n"
      "ds_read_b64_tr_b16 v[34:35], v25 offset:53504\n"
      "s_waitcnt lgkmcnt(8)\n"
      "v_mfma_f32_32x32x16_bf16 v[56:71], v[132:135], v[44:47], v[56:71]\n"
      "ds_read_b64_tr_b16 v[36:37], v25 offset:54272\n"
      "ds_read_b64_tr_b16 v[38:39], v25 offset:54528\n"
      "s_waitcnt lgkmcnt(8)\n"
      "v_mfma_f32_32x32x16_bf16 v[56:71], v[136:139], v[48:51], v[56:71]\n"
      "ds_read_b64_tr_b16 v[40:41], v25 offset:55296\n"
      "ds_read_b64_tr_b16 v[42:43], v25 offset:55552\n"
      "s_waitcnt lgkmcnt(8)\n"
      "v_mfma_f32_32x32x16_bf16 v[56:71], v[140:143], v[52:55], v[56:71]\n"
      "ds_read_b64_tr_b16 v[44:45], v25 offset:56320\n"
      "ds_read_b64_tr_b16 v[46:47], v25 offset:56576\n"
      "s_waitcnt lgkmcnt(6)\n"
      "v_mfma_f32_32x32x16_bf16 v[56:71], v[144:147], v[32:35], v[56:71]\n"
      "ds_read_b64_tr_b16 v[48:49], v25 offset:57344\n"
      "ds_read_b64_tr_b16 v[50:51], v25 offset:57600\n"
      "s_waitcnt lgkmcnt(6)\n"
      "v_mfma_f32_32x32x16_bf16 v[56:71], v[148:151], v[36:39], v[56:71]\n"
      "ds_read_b64_tr_b16 v[52:53], v25 offset:58368\n"
      "ds_read_b64_tr_b16 v[54:55], v25 offset:58624\n"
      "s_waitcnt lgkmcnt(6)\n"
      "v_mfma_f32_32x32x16_bf16 v[56:71], v[152:155], v[40:43], v[56:71]\n"
      "ds_read_b64_tr_b16 v[32:33], v25 offset:59392\n"
      "ds_read_b64_tr_b16 v[34:35], v25 offset:59648\n"
      "s_waitcnt lgkmcnt(6)\n"
      "v_mfma_f32_32x32x16_bf16 v[56:71], v[156:159], v[44:47], v[56:71]\n"
      "ds_read_b64_tr_b16 v[36:37], v25 offset:60416\n"
      "ds_read_b64_tr_b16 v[38:39], v25 offset:60672\n"
      "ds_read_b32 v28, %[fa]\n"
      "s_waitcnt lgkmcnt(7)\n"
      "v_mfma_f32_32x32x16_bf16 v[56:71], v[160:163], v[48:51], v[56:71]\n"
      "ds_read_b64_tr_b16 v[40:41], v25 offset:61440\n"
      "ds_read_b64_tr_b16 v[42:43], v25 offset:61696\n"
      "ds_read_b32 v28, %[fa]\n"
      "s_waitcnt lgkmcnt(8)\n"
      "v_mfma_f32_32x32x16_bf16 v[56:71], v[164:167], v[52:55], v[56:71]\n"
      "ds_read_b64_tr_b16 v[44:45], v25 offset:62464\n"
      "ds_read_b64_tr_b16 v[46:47], v25 offset:62720\n"
      "s_waitcnt lgkmcnt(8)\n"
      "v_mfma_f32_32x32x16_bf16 v[56:71], v[168:171], v[32:35], v[56:71]\n"
      "ds_read_b64_tr_b16 v[48:49], v25 offset:63488\n"
      "ds_read_b64_tr_b16 v[50:51], v25 offset:63744\n"
      "v_cmp_gt_u32 vcc, 14, v28\n"
      "s_waitcnt lgkmcnt(8)\n"
      "v_mfma_f32_32x32x16_bf16 v[56:71], v[172:175], v[36:39], v[56:71]\n"
      "s_cbranch_vccnz .LsV5_%=\n"
      ".LrV5_%=:\n"
      "ds_read_b64_tr_b16 v[208:209], %[wv] offset:32768\n"
      "ds_read_b64_tr_b16 v[210:211], %[wv] offset:33792\n"
      "ds_read_b64_tr_b16 v[52:53], v25 offset:64512\n"
      "ds_read_b64_tr_b16 v[54:55], v25 offset:64768\n"
      "v_cmp_gt_u32 vcc, 14, v28\n"
      "ds_read_b32 v28, %[fa]\n"
      "s_waitcnt lgkmcnt(10)\n"
      "v_mfma_f32_32x32x16_bf16 v[56:71], v[176:179], v[40:43], v[56:71]\n"
      "s_cbranch_vccz .LrS0b_%=\n"
      ".LsS0b_%=:\n"
      "ds_read_b32 v28, %[fa]\n"
      "s_addk_i32 %[fuel], -1\n"
      "s_cmp_lt_i32 %[fuel], 0\n"
      "s_waitcnt lgkmcnt(0)\n"
      "s_cbranch_scc1 .LrS0b_%=\n"
      "v_cmp_gt_u32 vcc, 14, v28\n"
      "s_cbranch_vccnz .LsS0b_%=\n"
      ".LrS0b_%=:\n"
      "ds_read_b64_tr_b16 v[212:213], %[wv] offset:36864\n"
      "ds_read_b64_tr_b16 v[214:215], %[wv] offset:37888\n"
      "ds_read_b64_tr_b16 v[32:33], %[sa] offset:0\n"
      "ds_read_b64_tr_b16 v[34:35], %[sa] offset:256\n"
      "s_waitcnt lgkmcnt(11)\n"
      "v_mfma_f32_32x32x16_bf16 v[56:71], v[180:183], v[44:47], v[56:71]\n"
      "ds_read_b64_tr_b16 v[216:217], %[wv] offset:40960\n"
      "ds_read_b64_tr_b16 v[218:219], %[wv] offset:41984\n"
      "ds_read_b64_tr_b16 v[36:37], %[sa] offset:1024\n"
      "ds_read_b64_tr_b16 v[38:39], %[sa] offset:1280\n"
      "s_waitcnt lgkmcnt(13)\n"
      "v_mfma_f32_32x32x16_bf16 v[56:71], v[184:187], v[48:51], v[56:71]\n"
      "ds_read_b64_tr_b16 v[220:221], %[wv] offset:45056\n"
      "ds_read_b64_tr_b16 v[222:223], %[wv] offset:46080\n"
      "ds_read_b64_tr_b16 v[40:41], %[sa] offset:2048\n"
      "ds_read_b64_tr_b16 v[42:43], %[sa] offset:2304\n"
      "v_cmp_gt_u32 vcc, 15, v28\n"
      "s_waitcnt lgkmcnt(13)\n"
      "v_mfma_f32_32x32x16_bf16 v[56:71], v[188:191], v[52:55], v[56:71]\n"
      "s_cbranch_vccnz .LsV6_%=\n"
      ".LrV6_%=:\n"
      "ds_read_b64_tr_b16 v[224:225], %[wv] offset:49152\n"
      "ds_read_b64_tr_b16 v[226:227], %[wv] offset:50176\n"
      "ds_read_b64_tr_b16 v[44:45], %[sa] offset:3072\n"
      "ds_read_b64_tr_b16 v[46:47], %[sa] offset:3328\n"
      "ds_read_b32 v28, %[fa]\n"
      "s_waitcnt lgkmcnt(13)\n"
      "v_mfma_f32_32x32x16_bf16 v[56:71], v[192:195], v[32:35], v[56:71]\n"
      "ds_read_b64_tr_b16 v[228:229], %[wv] offset:53248\n"
      "ds_read_b64_tr_b16 v[230:231], %[wv] offset:54272\n"
      "ds_read_b64_tr_b16 v[48:49], %[sa] offset:4096\n"
      "ds_read_b64_tr_b16 v[50:51], %[sa] offset:4352\n"
      "s_waitcnt lgkmcnt(13)\n"
      "v_mfma_f32_32x32x16_bf16 v[56:71], v[196:199], v[36:39], v[56:71]\n"
      "ds_read_b64_tr_b16 v[232:233], %[wv] offset:57344\n"
      "ds_read_b64_tr_b16 v[234:235], %[wv] offset:58368\n"
      "ds_read_b64_tr_b16 v[52:53], %[sa] offset:5120\n"
      "ds_read_b64_tr_b16 v[54:55], %[sa] offset:5376\n"
      "s_waitcnt lgkmcnt(13)\n"
      "v_mfma_f32_32x32x16_bf16 v[56:71], v[200:203], v[40:43], v[56:71]\n"
      "ds_read_b64_tr_b16 v[236:237], %[wv] offset:61440\n"
      "ds_read_b64_tr_b16 v[238:239], %[wv] offset:62464\n"
      "ds_read_b64_tr_b16 v[32:33], %[sa] offset:6144\n"
      "ds_read_b64_tr_b16 v[34:35], %[sa] offset:6400\n"
      "v_cmp_gt_u32 vcc, 16, v28\n"
      "s_waitcnt lgkmcnt(13)\n"
      "v_mfma_f32_32x32x16_bf16 v[56:71], v[204:207], v[44:47], v[56:71]\n"
      "s_cbranch_vccnz .LsV7_%=\n"
      ".LrV7_%=:\n"
      "ds_read_b64_tr_b16 v[240:241], %[wv] offset:16384\n"
      "ds_read_b64_tr_b16 v[242:243], %[wv] offset:17408\n"
      "ds_read_b64_tr_b16 v[36:37], %[sa] offset:7168\n"
      "ds_read_b64_tr_b16 v[38:39], %[sa] offset:7424\n"
      "ds_add_u32 %[sg], v27\n"
      "s_waitcnt lgkmcnt(13)\n"
      "v_mfma_f32_32x32x16_bf16 v[56:71], v[208:211], v[48:51], v[56:71]\n"
      "ds_read_b64_tr_b16 v[244:245], %[wv] offset:20480\n"
      "ds_read_b64_tr_b16 v[246:247], %[wv] offset:21504\n"
      "ds_read_b64_tr_b16 v[40:41], %[sa] offset:8192\n"
      "ds_read_b64_tr_b16 v[42:43], %[sa] offset:8448\n"
      "s_waitcnt lgkmcnt(13)\n"
      "v_mfma_f32_32x32x16_bf16 v[56:71], v[212:215], v[52:55], v[56:71]\n"
      "ds_read_b64_tr_b16 v[248:249], %[wv] offset:24576\n"
      "ds_read_b64_tr_b16 v[250:251], %[wv] offset:25600\n"
      "ds_read_b64_tr_b16 v[44:45], %[sa] offset:9216\n"
      "ds_read_b64_tr_b16 v[46:47], %[sa] offset:9472\n"
      "s_waitcnt lgkmcnt(13)\n"
      "v_mfma_f32_32x32x16_bf16 v[56:71], v[216:219], v[32:35], v[56:71]\n"
      "ds_read_b64_tr_b16 v[252:253], %[wv] offset:28672\n"
      "ds_read_b64_tr_b16 v[254:255], %[wv] offset:29696\n"
      "ds_read_b64_tr_b16 v[48:49], %[sa] offset:10240\n"
      "ds_read_b64_tr_b16 v[50:51], %[sa] offset:10496\n"
      "s_waitcnt lgkmcnt(13)\n"
      "v_mfma_f32_32x32x16_bf16 v[56:71], v[220:223], v[36:39], v[56:71]\n"
      "ds_read_b64_tr_b16 v[52:53], %[sa] offset:11264\n"
      "ds_read_b64_tr_b16 v[54:55], %[sa] offset:11520\n"
      "s_waitcnt lgkmcnt(10)\n"
      "v_mfma_f32_32x32x16_bf16 v[56:71], v[224:227], v[40:43], v[56:71]\n"
      "ds_read_b64_tr_b16 v[32:33], %[sa] offset:12288\n"
      "ds_read_b64_tr_b16 v[34:35], %[sa] offset:12544\n"
      "ds_read_b32 v28, %[fa]\n"
      "s_waitcnt lgkmcnt(9)\n"
      "v_mfma_f32_32x32x16_bf16 v[56:71], v[228:231], v[44:47], v[56:71]\n"
      "ds_read_b64_tr_b16 v[36:37], %[sa] offset:13312\n"
      "ds_read_b64_tr_b16 v[38:39], %[sa] offset:13568\n"
      "s_waitcnt lgkmcnt(7)\n"
      "v_mfma_f32_32x32x16_bf16 v[56:71], v[232:235], v[48:51], v[56:71]\n"
      "ds_read_b64_tr_b16 v[40:41], %[sa] offset:14336\n"
      "ds_read_b64_tr_b16 v[42:43], %[sa] offset:14592\n"
      "s_waitcnt lgkmcnt(7)\n"
      "v_mfma_f32_32x32x16_bf16 v[56:71], v[236:239], v[52:55], v[56:71]\n"
      "ds_read_b64_tr_b16 v[44:45], %[sa] offset:15360\n"
      "ds_read_b64_tr_b16 v[46:47], %[sa] offset:15616\n"
      "v_cmp_gt_u32 vcc, 17, v28\n"
      "s_waitcnt lgkmcnt(7)\n"
      "v_mfma_f32_32x32x16_bf16 v[56:71], v[240:243], v[32:35], v[56:71]\n"
      "s_cbranch_vccz .LrS1a_%=\n"
      ".LsS1a_%=:\n"
      "ds_read_b32 v28, %[fa]\n"
      "s_addk_i32 %[fuel], -1\n"
      "s_cmp_lt_i32 %[fuel], 0\n"
      "s_waitcnt lgkmcnt(0)\n"
      "s_cbranch_scc1 .LrS1a_%=\n"
      "v_cmp_gt_u32 vcc, 17, v28\n"
      "s_cbranch_vccnz .LsS1a_%=\n"
      ".LrS1a_%=:\n"
      "ds_read_b64_tr_b16 v[48:49], v25 offset:0\n"
      "ds_read_b64_tr_b16 v[50:51], v25 offset:256\n"
      "s_waitcnt lgkmcnt(6)\n"
      "v_mfma_f32_32x32x16_bf16 v[56:71], v[244:247], v[36:39], v[56:71]\n"
      "ds_read_b64_tr_b16 v[52:53], v25 offset:1024\n"
      "ds_read_b64_tr_b16 v[54:55], v25 offset:1280\n"
      "s_waitcnt lgkmcnt(6)\n"
      "v_mfma_f32_32x32x16_bf16 v[56:71], v[248:251], v[40:43], v[56:71]\n"
      "ds_read_b64_tr_b16 v[32:33], v25 offset:2048\n"
      "ds_read_b64_tr_b16 v[34:35], v25 offset:2304\n"
      "s_waitcnt lgkmcnt(6)\n"
      "v_mfma_f32_32x32x16_bf16 v[56:71], v[252:255], v[44:47], v[56:71]\n"
      "ds_read_b64_tr_b16 v[36:37], v25 offset:3072\n"
      "ds_read_b64_tr_b16 v[38:39], v25 offset:3328\n"
      "s_waitcnt lgkmcnt(6)\n"
      "v_mfma_f32_32x32x16_bf16 v[72:87], v[128:131], v[48:51], 0\n"
      "ds_read_b64_tr_b16 v[40:41], v25 offset:4096\n"
      "ds_read_b64_tr_b16 v[42:43], v25 offset:4352\n"
      "s_waitcnt lgkmcnt(6)\n"
      "v_mfma_f32_32x32x16_bf16 v[72:87], v[132:135], v[52:55], v[72:87]\n"
      "ds_read_b64_tr_b16 v[44:45], v25 offset:5120\n"
      "ds_read_b64_tr_b16 v[46:47], v25 offset:5376\n"
      "s_waitcnt lgkmcnt(6)\n"
      "v_mfma_f32_32x32x16_bf16 v[72:87], v[136:139], v[32:35], v[72:87]\n"
      "ds_read_b64_tr_b16 v[48:49], v25 offset:6144\n"
      "ds_read_b64_tr_b16 v[50:51], v25 offset:6400\n"
      "s_waitcnt lgkmcnt(6)\n"
      "v_mfma_f32_32x32x16_bf16 v[72:87], v[140:143], v[36:39], v[72:87]\n"
      "ds_read_b64_tr_b16 v[52:53], v25 offset:7168\n"
      "ds_read_b64_tr_b16 v[54:55], v25 offset:7424\n"
      "v_cvt_pk_bf16_f32 v56, v56, v57\n"
      "v_cvt_pk_bf16_f32 v57, v58, v59\n"
      "s_waitcnt lgkmcnt(6)\n"
      "v_mfma_f32_32x32x16_bf16 v[72:87], v[144:147], v[40:43], v[72:87]\n"
      "ds_read_b64_tr_b16 v[32:33], v25 offset:8192\n"
      "ds_read_b64_tr_b16 v[34:35], v25 offset:8448\n"
      "v_cvt_pk_bf16_f32 v58, v60, v61\n"
      "v_cvt_pk_bf16_f32 v59, v62, v63\n"
      "s_waitcnt lgkmcnt(6)\n"
      "v_mfma_f32_32x32x16_bf16 v[72:87], v[148:151], v[44:47], v[72:87]\n"
      "ds_read_b64_tr_b16 v[36:37], v25 offset:9216\n"
      "ds_read_b64_tr_b16 v[38:39], v25 offset:9472\n"
      "ds_write_b128 %[wr], v[56:59]\n"
      "v_cvt_pk_bf16_f32 v60, v64, v65\n"
      "s_waitcnt lgkmcnt(7)\n"
      "v_mfma_f32_32x32x16_bf16 v[72:87], v[152:155], v[48:51], v[72:87]\n"
      "ds_read_b64_tr_b16 v[40:41], v25 offset:10240\n"
      "ds_read_b64_tr_b16 v[42:43], v25 offset:10496\n"
      "v_cvt_pk_bf16_f32 v61, v66, v67\n"
      "v_cvt_pk_bf16_f32 v62, v68, v69\n"
      "s_waitcnt lgkmcnt(7)\n"
      "v_mfma_f32_32x32x16_bf16 v[72:87], v[156:159], v[52:55], v[72:87]\n"
      "ds_read_b64_tr_b16 v[44:45], v25 offset:11264\n"
      "ds_read_b64_tr_b16 v[46:47], v25 offset:11520\n"
      "v_cvt_pk_bf16_f32 v63, v70, v71\n"
      "ds_write_b128 v26, v[60:63]\n"
      "s_waitcnt lgkmcnt(8)\n"
      "v_mfma_f32_32x32x16_bf16 v[72:87], v[160:163], v[32:35], v[72:87]\n"
      "ds_read_b64_tr_b16 v[48:49], v25 offset:12288\n"
      "ds_read_b64_tr_b16 v[50:51], v25 offset:12544\n"
      "ds_read_b32 v28, %[fa]\n"
      "ds_read_b128 v[64:67], %[rd] offset:0\n"
      "s_waitcnt lgkmcnt(10)\n"
      "v_mfma_f32_32x32x16_bf16 v[72:87], v[164:167], v[36:39], v[72:87]\n"
      "ds_read_b64_tr_b16 v[52:53], v25 offset:13312\n"
      "ds_read_b64_tr_b16 v[54:55], v25 offset:13568\n"
      "ds_read_b128 v[68:71], %[rd] offset:4096\n"
      "s_waitcnt lgkmcnt(10)\n"
      "v_mfma_f32_32x32x16_bf16 v[72:87], v[168:171], v[40:43], v[72:87]\n"
      "ds_read_b64_tr_b16 v[32:33], v25 offset:14336\n"
      "ds_read_b64_tr_b16 v[34:35], v25 offset:14592\n"
      "s_waitcnt lgkmcnt(10)\n"
      "v_mfma_f32_32x32x16_bf16 v[72:87], v[172:175], v[44:47], v[72:87]\n"
      "ds_read_b64_tr_b16 v[36:37], v25 offset:15360\n"
      "ds_read_b64_tr_b16 v[38:39], v25 offset:15616\n"
      "v_cmp_gt_u32 vcc, 18, v28\n"
      "s_waitcnt lgkmcnt(9)\n"
      "v_mfma_f32_32x32x16_bf16 v[72:87], v[176:179], v[48:51], v[72:87]\n"
      "s_cbranch_vccz .LrS1b_%=\n"
      ".LsS1b_%=:\n"
      "ds_read_b32 v28, %[fa]\n"
      "s_addk_i32 %[fuel], -1\n"
      "s_cmp_lt_i32 %[fuel], 0\n"
      "s_waitcnt lgkmcnt(0)\n"
      "s_cbranch_scc1 .LrS1b_%=\n"
      "v_cmp_gt_u32 vcc, 18, v28\n"
      "s_cbranch_vccnz .LsS1b_%=\n"
      ".LrS1b_%=:\n"
      "ds_read_b64_tr_b16 v[40:41], v25 offset:16384\n"
      "ds_read_b64_tr_b16 v[42:43], v25 offset:16640\n"
      "s_waitcnt lgkmcnt(7)\n"
      "v_mfma_f32_32x32x16_bf16 v[72:87], v[180:183], v[52:55], v[72:87]\n"
      "ds_read_b64_tr_b16 v[44:45], v25 offset:17408\n"
      "ds_read_b64_tr_b16 v[46:47], v25 offset:17664\n"
      "s_waitcnt lgkmcnt(6)\n"
      "v_mfma_f32_32x32x16_bf16 v[72:87], v[184:187], v[32:35], v[72:87]\n"
      "ds_read_b64_tr_b16 v[48:49], v25 offset:18432\n"
      "ds_read_b64_tr_b16 v[50:51], v25 offset:18688\n"
      "global_store_dwordx4 %[oo], v[64:67], s[92:93] sc1\n"
      "s_add_u32 s92, s92, 16384\n"
      "s_addc_u32 s93, s93, 0\n"
      "s_waitcnt lgkmcnt(6)\n"
      "v_mfma_f32_32x32x16_bf16 v[72:87], v[188:191], v[36:39], v[72:87]\n"
      "ds_read_b64_tr_b16 v[52:53], v25 offset:19456\n"
      "ds_read_b64_tr_b16 v[54:55], v25 offset:19712\n"
      "global_store_dwordx4 %[oo], v[68:71], s[92:93] sc1\n"
      "s_add_u32 s92, s92, 49152\n"
      "s_addc_u32 s93, s93, 0\n"
      "s_waitcnt lgkmcnt(6)\n"
      "v_mfma_f32_32x32x16_bf16 v[72:87], v[192:195], v[40:43], v[72:87]\n"
      "ds_read_b64_tr_b16 v[32:33], v25 offset:20480\n"
      "ds_read_b64_tr_b16 v[34:35], v25 offset:20736\n"
      "s_waitcnt lgkmcnt(6)\n"
      "v_mfma_f32_32x32x16_bf16 v[72:87], v[196:199], v[44:47], v[72:87]\n"
      "ds_read_b64_tr_b16 v[36:37], v25 offset:21504\n"
      "ds_read_b64_tr_b16 v[38:39], v25 offset:21760\n"
      "s_waitcnt lgkmcnt(6)\n"
      "v_mfma_f32_32x32x16_bf16 v[72:87], v[200:203], v[48:51], v[72:87]\n"
      "ds_read_b64_tr_b16 v[40:41], v25 offset:22528\n"
      "ds_read_b64_tr_b16 v[42:43], v25 offset:22784\n"
      "s_waitcnt lgkmcnt(6)\n"
      "v_mfma_f32_32x32x16_bf16 v[72:87], v[204:207], v[52:55], v[72:87]\n"
      "ds_read_b64_tr_b16 v[44:45], v25 offset:23552\n"
      "ds_read_b64_tr_b16 v[46:47], v25 offset:23808\n"
      "s_waitcnt lgkmcnt(6)\n"
      "v_mfma_f32_32x32x16_bf16 v[72:87], v[208:211], v[32:35], v[72:87]\n"
      "ds_read_b64_tr_b16 v[48:49], v25 offset:24576\n"
      "ds_read_b64_tr_b16 v[50:51], v25 offset:24832\n"
      "s_waitcnt lgkmcnt(6)\n"
      "v_mfma_f32_32x32x16_bf16 v[72:87], v[212:215], v[36:39], v[72:87]\n"
      "ds_read_b64_tr_b16 v[52:53], v25 offset:25600\n"
      "ds_read_b64_tr_b16 v[54:55], v25 offset:25856\n"
      "s_waitcnt lgkmcnt(6)\n"
      "v_mfma_f32_32x32x16_bf16 v[72:87], v[216:219], v[40:43], v[72:87]\n"
      "ds_read_b64_tr_b16 v[32:33], v25 offset:26624\n"
      "ds_read_b64_tr_b16 v[34:35], v25 offset:26880\n"
      "s_waitcnt lgkmcnt(6)\n"
      "v_mfma_f32_32x32x16_bf16 v[72:87], v[220:223], v[44:47], v[72:87]\n"
      "ds_read_b64_tr_b16 v[36:37], v25 offset:27648\n"
      "ds_read_b64_tr_b16 v[38:39], v25 offset:27904\n"
      "s_waitcnt lgkmcnt(6)\n"
      "v_mfma_f32_32x32x16_bf16 v[72:87], v[224:227], v[48:51], v[72:87]\n"
      "ds_read_b64_tr_b16 v[40:41], v25 offset:28672\n"
      "ds_read_b64_tr_b16 v[42:43], v25 offset:28928\n"
      "ds_read_b32 v28, %[fa]\n"
      "s_waitcnt lgkmcnt(7)\n"
      "v_mfma_f32_32x32x16_bf16 v[72:87], v[228:231], v[52:55], v[72:87]\n"
      "ds_read_b64_tr_b16 v[44:45], v25 offset:29696\n"
      "ds_read_b64_tr_b16 v[46:47], v25 offset:29952\n"
      "s_waitcnt lgkmcnt(7)\n"
      "v_mfma_f32_32x32x16_bf16 v[72:87], v[232:235], v[32:35], v[72:87]\n"
      "ds_read_b64_tr_b16 v[48:49], v25 offset:30720\n"
      "ds_read_b64_tr_b16 v[50:51], v25 offset:30976\n"
      "s_waitcnt lgkmcnt(7)\n"
      "v_mfma_f32_32x32x16_bf16 v[72:87], v[236:239], v[36:39], v[72:87]\n"
      "ds_read_b64_tr_b16 v[52:53], v25 offset:31744\n"
      "ds_read_b64_tr_b16 v[54:55], v25 offset:32000\n"
      "v_cmp_gt_u32 vcc, 19, v28\n"
      "s_waitcnt lgkmcnt(7)\n"
      "v_mfma_f32_32x32x16_bf16 v[72:87], v[240:243], v[40:43], v[72:87]\n"
      "s_cbranch_vccz .LrS2a_%=\n"
      ".LsS2a_%=:\n"
      "ds_read_b32 v28, %[fa]\n"
      "s_addk_i32 %[fuel], -1\n"
      "s_cmp_lt_i32 %[fuel], 0\n"
      "s_waitcnt lgkmcnt(0)\n"
      "s_cbranch_scc1 .LrS2a_%=\n"
      "v_cmp_gt_u32 vcc, 19, v28\n"
      "s_cbranch_vccnz .LsS2a_%=\n"
      ".LrS2a_%=:\n"
      "ds_read_b64_tr_b16 v[32:33], v25 offset:32768\n"
      "ds_read_b64_tr_b16 v[34:35], v25 offset:33024\n"
      "s_waitcnt lgkmcnt(6)\n"
      "v_mfma_f32_32x32x16_bf16 v[72:87], v[244:247], v[44:47], v[72:87]\n"
      "ds_read_b64_tr_b16 v[36:37], v25 offset:33792\n"
      "ds_read_b64_tr_b16 v[38:39], v25 offset:34048\n"
      "s_waitcnt lgkmcnt(6)\n"
      "v_mfma_f32_32x32x16_bf16 v[72:87], v[248:251], v[48:51], v[72:87]\n"
      "ds_read_b64_tr_b16 v[40:41], v25 offset:34816\n"
      "ds_read_b64_tr_b16 v[42:43], v25 offset:35072\n"
      "s_waitcnt lgkmcnt(6)\n"
      "v_mfma_f32_32x32x16_bf16 v[72:87], v[252:255], v[52:55], v[72:87]\n"
      "ds_read_b64_tr_b16 v[44:45], v25 offset:35840\n"
      "ds_read_b64_tr_b16 v[46:47], v25 offset:36096\n"
      "s_waitcnt lgkmcnt(6)\n"
      "v_mfma_f32_32x32x16_bf16 v[88:103], v[128:131], v[32:35], 0\n"
      "ds_read_b64_tr_b16 v[48:49], v25 offset:36864\n"
      "ds_read_b64_tr_b16 v[50:51], v25 offset:37120\n"
      "s_waitcnt lgkmcnt(6)\n"
      "v_mfma_f32_32x32x16_bf16 v[88:103], v[132:135], v[36:39], v[88:103]\n"
      "ds_read_b64_tr_b16 v[52:53], v25 offset:37888\n"
      "ds_read_b64_tr_b16 v[54:55], v25 offset:38144\n"
      "s_waitcnt lgkmcnt(6)\n"
      "v_mfma_f32_32x32x16_bf16 v[88:103], v[136:139], v[40:43], v[88:103]\n"
      "ds_read_b64_tr_b16 v[32:33], v25 offset:38912\n"
      "ds_read_b64_tr_b16 v[34:35], v25 offset:39168\n"
      "s_waitcnt lgkmcnt(6)\n"
      "v_mfma_f32_32x32x16_bf16 v[88:103], v[140:143], v[44:47], v[88:103]\n"
      "ds_read_b64_tr_b16 v[36:37], v25 offset:39936\n"
      "ds_read_b64_tr_b16 v[38:39], v25 offset:40192\n"
      "v_cvt_pk_bf16_f32 v72, v72, v73\n"
      "v_cvt_pk_bf16_f32 v73, v74, v75\n"
      "s_waitcnt lgkmcnt(6)\n"
      "v_mfma_f32_32x32x16_bf16 v[88:103], v[144:147], v[48:51], v[88:103]\n"
      "ds_read_b64_tr_b16 v[40:41], v25 offset:40960\n"
      "ds_read_b64_tr_b16 v[42:43], v25 offset:41216\n"
      "v_cvt_pk_bf16_f32 v74, v76, v77\n"
      "v_cvt_pk_bf16_f32 v75, v78, v79\n"
      "s_waitcnt lgkmcnt(6)\n"
      "v_mfma_f32_32x32x16_bf16 v[88:103], v[148:151], v[52:55], v[88:103]\n"
      "ds_read_b64_tr_b16 v[44:45], v25 offset:41984\n"
      "ds_read_b64_tr_b16 v[46:47], v25 offset:42240\n"
      "ds_write_b128 %[wr], v[72:75]\n"
      "v_cvt_pk_bf16_f32 v76, v80, v81\n"
      "s_waitcnt lgkmcnt(7)\n"
      "v_mfma_f32_32x32x16_bf16 v[88:103], v[152:155], v[32:35], v[88:103]\n"
      "ds_read_b64_tr_b16 v[48:49], v25 offset:43008\n"
      "ds_read_b64_tr_b16 v[50:51], v25 offset:43264\n"
      "v_cvt_pk_bf16_f32 v77, v82, v83\n"
      "v_cvt_pk_bf16_f32 v78, v84, v85\n"
      "s_waitcnt lgkmcnt(7)\n"
      "v_mfma_f32_32x32x16_bf16 v[88:103], v[156:159], v[36:39], v[88:103]\n"
      "ds_read_b64_tr_b16 v[52:53], v25 offset:44032\n"
      "ds_read_b64_tr_b16 v[54:55], v25 offset:44288\n"
      "v_cvt_pk_bf16_f32 v79, v86, v87\n"
      "ds_write_b128 v26, v[76:79]\n"
      "s_waitcnt lgkmcnt(8)\n"
      "v_mfma_f32_32x32x16_bf16 v[88:103], v[160:163], v[40:43], v[88:103]\n"
      "ds_read_b64_tr_b16 v[32:33], v25 offset:45056\n"
      "ds_read_b64_tr_b16 v[34:35], v25 offset:45312\n"
      "ds_read_b32 v28, %[fa]\n"
      "ds_read_b128 v[80:83], %[rd] offset:0\n"
      "s_waitcnt lgkmcnt(10)\n"
      "v_mfma_f32_32x32x16_bf16 v[88:103], v[164:167], v[44:47], v[88:103]\n"
      "ds_read_b64_tr_b16 v[36:37], v25 offset:46080\n"
      "ds_read_b64_tr_b16 v[38:39], v25 offset:46336\n"
      "ds_read_b128 v[84:87], %[rd] offset:4096\n"
      "s_waitcnt lgkmcnt(10)\n"
      "v_mfma_f32_32x32x16_bf16 v[88:103], v[168:171], v[48:51], v[88:103]\n"
      "ds_read_b64_tr_b16 v[40:41], v25 offset:47104\n"
      "ds_read_b64_tr_b16 v[42:43], v25 offset:47360\n"
      "s_waitcnt lgkmcnt(10)\n"
      "v_mfma_f32_32x32x16_bf16 v[88:103], v[172:175], v[52:55], v[88:103]\n"
      "ds_read_b64_tr_b16 v[44:45], v25 offset:48128\n"
      "ds_read_b64_tr_b16 v[46:47], v25 offset:48384\n"
      "v_cmp_gt_u32 vcc, 20, v28\n"
      "s_waitcnt lgkmcnt(9)\n"
      "v_mfma_f32_32x32x16_bf16 v[88:103], v[176:179], v[32:35], v[88:103]\n"
      "s_cbranch_vccz .LrS2b_%=\n"
      ".LsS2b_%=:\n"
      "ds_read_b32 v28, %[fa]\n"
      "s_addk_i32 %[fuel], -1\n"
      "s_cmp_lt_i32 %[fuel], 0\n"
      "s_waitcnt lgkmcnt(0)\n"
      "s_cbranch_scc1 .LrS2b_%=\n"
      "v_cmp_gt_u32 vcc, 20, v28\n"
      "s_cbranch_vccnz .LsS2b_%=\n"
      ".LrS2b_%=:\n"
      "ds_read_b64_tr_b16 v[48:49], %[sa] offset:32768\n"
      "ds_read_b64_tr_b16 v[50:51], %[sa] offset:33024\n"
      "s_waitcnt lgkmcnt(7)\n"
      "v_mfma_f32_32x32x16_bf16 v[88:103], v[180:183], v[36:39], v[88:103]\n"
      "ds_read_b64_tr_b16 v[52:53], %[sa] offset:33792\n"
      "ds_read_b64_tr_b16 v[54:55], %[sa] offset:34048\n"
      "s_waitcnt lgkmcnt(6)\n"
      "v_mfma_f32_32x32x16_bf16 v[88:103], v[184:187], v[40:43], v[88:103]\n"
      "ds_read_b64_tr_b16 v[32:33], %[sa] offset:34816\n"
      "ds_read_b64_tr_b16 v[34:35], %[sa] offset:35072\n"
      "global_store_dwordx4 %[oo], v[80:83], s[92:93] sc1\n"
      "s_add_u32 s92, s92, 16384\n"
      "s_addc_u32 s93, s93, 0\n"
      "s_waitcnt lgkmcnt(6)\n"
      "v_mfma_f32_32x32x16_bf16 v[88:103], v[188:191], v[44:47], v[88:103]\n"
      "ds_read_b64_tr_b16 v[36:37], %[sa] offset:35840\n"
      "ds_read_b64_tr_b16 v[38:39], %[sa] offset:36096\n"
      "global_store_dwordx4 %[oo], v[84:87], s[92:93] sc1\n"
      "s_add_u32 s92, s92, 49152\n"
      "s_addc_u32 s93, s93, 0\n"
      "s_waitcnt lgkmcnt(6)\n"
      "v_mfma_f32_32x32x16_bf16 v[88:103], v[192:195], v[48:51], v[88:103]\n"
      "ds_read_b64_tr_b16 v[40:41], %[sa] offset:36864\n"
      "ds_read_b64_tr_b16 v[42:43], %[sa] offset:37120\n"
      "s_waitcnt lgkmcnt(6)\n"
      "v_mfma_f32_32x32x16_bf16 v[88:103], v[196:199], v[52:55], v[88:103]\n"
      "ds_read_b64_tr_b16 v[44:45], %[sa] offset:37888\n"
      "ds_read_b64_tr_b16 v[46:47], %[sa] offset:38144\n"
      "s_waitcnt lgkmcnt(6)\n"
      "v_mfma_f32_32x32x16_bf16 v[88:103], v[200:203], v[32:35], v[88:103]\n"
      "ds_read_b64_tr_b16 v[48:49], %[sa] offset:38912\n"
      "ds_read_b64_tr_b16 v[50:51], %[sa] offset:39168\n"
      "s_waitcnt lgkmcnt(6)\n"
      "v_mfma_f32_32x32x16_bf16 v[88:103], v[204:207], v[36:39], v[88:103]\n"
      "ds_read_b64_tr_b16 v[52:53], %[sa] offset:39936\n"
      "ds_read_b64_tr_b16 v[54:55], %[sa] offset:40192\n"
      "s_waitcnt lgkmcnt(6)\n"
      "v_mfma_f32_32x32x16_bf16 v[88:103], v[208:211], v[40:43], v[88:103]\n"
      "ds_read_b64_tr_b16 v[32:33], %[sa] offset:40960\n"
      "ds_read_b64_tr_b16 v[34:35], %[sa] offset:41216\n"
      "s_waitcnt lgkmcnt(6)\n"
      "v_mfma_f32_32x32x16_bf16 v[88:103], v[212:215], v[44:47], v[88:103]\n"
      "ds_read_b64_tr_b16 v[36:37], %[sa] offset:41984\n"
      "ds_read_b64_tr_b16 v[38:39], %[sa] offset:42240\n"
      "s_waitcnt lgkmcnt(6)\n"
      "v_mfma_f32_32x32x16_bf16 v[88:103], v[216:219], v[48:51], v[88:103]\n"
      "ds_read_b64_tr_b16 v[40:41], %[sa] offset:43008\n"
      "ds_read_b64_tr_b16 v[42:43], %[sa] offset:43264\n"
      "s_waitcnt lgkmcnt(6)\n"
      "v_mfma_f32_32x32x16_bf16 v[88:103], v[220:223], v[52:55], v[88:103]\n"
      "ds_read_b64_tr_b16 v[44:45], %[sa] offset:44032\n"
      "ds_read_b64_tr_b16 v[46:47], %[sa] offset:44288\n"
      "s_waitcnt lgkmcnt(6)\n"
      "v_mfma_f32_32x32x16_bf16 v[88:103], v[224:227], v[32:35], v[88:103]\n"
      "ds_read_b64_tr_b16 v[48:49], %[sa] offset:45056\n"
      "ds_read_b64_tr_b16 v[50:51], %[sa] offset:45312\n"
      "ds_read_b32 v28, %[fa]\n"
      "s_waitcnt lgkmcnt(7)\n"
      "v_mfma_f32_32x32x16_bf16 v[88:103], v[228:231], v[36:39], v[88:103]\n"
      "ds_read_b64_tr_b16 v[52:53], %[sa] offset:46080\n"
      "ds_read_b64_tr_b16 v[54:55], %[sa] offset:46336\n"
      "s_waitcnt lgkmcnt(7)\n"
      "v_mfma_f32_32x32x16_bf16 v[88:103], v[232:235], v[40:43], v[88:103]\n"
      "ds_read_b64_tr_b16 v[32:33], %[sa] offset:47104\n"
      "ds_read_b64_tr_b16 v[34:35], %[sa] offset:47360\n"
      "s_waitcnt lgkmcnt(7)\n"
      "v_mfma_f32_32x32x16_bf16 v[88:103], v[236:239], v[44:47], v[88:103]\n"
      "ds_read_b64_tr_b16 v[36:37], %[sa] offset:48128\n"
      "ds_read_b64_tr_b16 v[38:39], %[sa] offset:48384\n"
      "v_cmp_gt_u32 vcc, 21, v28\n"
      "s_waitcnt lgkmcnt(7)\n"
      "v_mfma_f32_32x32x16_bf16 v[88:103], v[240:243], v[48:51], v[88:103]\n"
      "s_cbranch_vccz .LrS3a_%=\n"
      ".LsS3a_%=:\n"
      "ds_read_b32 v28, %[fa]\n"
      "s_addk_i32 %[fuel], -1\n"
      "s_cmp_lt_i32 %[fuel], 0\n"
      "s_waitcnt lgkmcnt(0)\n"
      "s_cbranch_scc1 .LrS3a_%=\n"
      "v_cmp_gt_u32 vcc, 21, v28\n"
      "s_cbranch_vccnz .LsS3a_%=\n"
      ".LrS3a_%=:\n"
      "ds_read_b64_tr_b16 v[40:41], %[sa] offset:49152\n"
      "ds_read_b64_tr_b16 v[42:43], %[sa] offset:49408\n"
      "s_waitcnt lgkmcnt(6)\n"
      "v_mfma_f32_32x32x16_bf16 v[88:103], v[244:247], v[52:55], v[88:103]\n"
      "ds_read_b64_tr_b16 v[44:45], %[sa] offset:50176\n"
      "ds_read_b64_tr_b16 v[46:47], %[sa] offset:50432\n"
      "s_waitcnt lgkmcnt(6)\n"
      "v_mfma_f32_32x32x16_bf16 v[88:103], v[248:251], v[32:35], v[88:103]\n"
      "ds_read_b64_tr_b16 v[48:49], %[sa] offset:51200\n"
      "ds_read_b64_tr_b16 v[50:51], %[sa] offset:51456\n"
      "s_waitcnt lgkmcnt(6)\n"
      "v_mfma_f32_32x32x16_bf16 v[88:103], v[252:255], v[36:39], v[88:103]\n"
      "ds_read_b64_tr_b16 v[52:53], %[sa] offset:52224\n"
      "ds_read_b64_tr_b16 v[54:55], %[sa] offset:52480\n"
      "s_waitcnt lgkmcnt(6)\n"
      "v_mfma_f32_32x32x16_bf16 v[112:127], v[128:131], v[40:43], 0\n"
      "ds_read_b64_tr_b16 v[32:33], %[sa] offset:53248\n"
      "ds_read_b64_tr_b16 v[34:35], %[sa] offset:53504\n"
      "s_waitcnt lgkmcnt(6)\n"
      "v_mfma_f32_32x32x16_bf16 v[112:127], v[132:135], v[44:47], v[112:127]\n"
      "ds_read_b64_tr_b16 v[36:37], %[sa] offset:54272\n"
      "ds_read_b64_tr_b16 v[38:39], %[sa] offset:54528\n"
      "s_waitcnt lgkmcnt(6)\n"
      "v_mfma_f32_32x32x16_bf16 v[112:127], v[136:139], v[48:51], v[112:127]\n"
      "ds_read_b64_tr_b16 v[40:41], %[sa] offset:55296\n"
      "ds_read_b64_tr_b16 v[42:43], %[sa] offset:55552\n"
      "s_waitcnt lgkmcnt(6)\n"
      "v_mfma_f32_32x32x16_bf16 v[112:127], v[140:143], v[52:55], v[112:127]\n"
      "ds_read_b64_tr_b16 v[44:45], %[sa] offset:56320\n"
      "ds_read_b64_tr_b16 v[46:47], %[sa] offset:56576\n"
      "v_cvt_pk_bf16_f32 v88, v88, v89\n"
      "v_cvt_pk_bf16_f32 v89, v90, v91\n"
      "s_waitcnt lgkmcnt(6)\n"
      "v_mfma_f32_32x32x16_bf16 v[112:127], v[144:147], v[32:35], v[112:127]\n"
      "ds_read_b64_tr_b16 v[48:49], %[sa] offset:57344\n"
      "ds_read_b64_tr_b16 v[50:51], %[sa] offset:57600\n"
      "v_cvt_pk_bf16_f32 v90, v92, v93\n"
      "v_cvt_pk_bf16_f32 v91, v94, v95\n"
      "s_waitcnt lgkmcnt(6)\n"
      "v_mfma_f32_32x32x16_bf16 v[112:127], v[148:151], v[36:39], v[112:127]\n"
      "ds_read_b64_tr_b16 v[52:53], %[sa] offset:58368\n"
      "ds_read_b64_tr_b16 v[54:55], %[sa] offset:58624\n"
      "ds_write_b128 %[wr], v[88:91]\n"
      "v_cvt_pk_bf16_f32 v92, v96, v97\n"
      "s_waitcnt lgkmcnt(7)\n"
      "v_mfma_f32_32x32x16_bf16 v[112:127], v[152:155], v[40:43], v[112:127]\n"
      "ds_read_b64_tr_b16 v[32:33], %[sa] offset:59392\n"
      "ds_read_b64_tr_b16 v[34:35], %[sa] offset:59648\n"
      "v_cvt_pk_bf16_f32 v93, v98, v99\n"
      "v_cvt_pk_bf16_f32 v94, v100, v101\n"
      "s_waitcnt lgkmcnt(7)\n"
      "v_mfma_f32_32x32x16_bf16 v[112:127], v[156:159], v[44:47], v[112:127]\n"
      "ds_read_b64_tr_b16 v[36:37], %[sa] offset:60416\n"
      "ds_read_b64_tr_b16 v[38:39], %[sa] offset:60672\n"
      "v_cvt_pk_bf16_f32 v95, v102, v103\n"
      "ds_write_b128 v26, v[92:95]\n"
      "s_waitcnt lgkmcnt(8)\n"
      "v_mfma_f32_32x32x16_bf16 v[112:127], v[160:163], v[48:51], v[112:127]\n"
      "ds_read_b64_tr_b16 v[40:41], %[sa] offset:61440\n"
      "ds_read_b64_tr_b16 v[42:43], %[sa] offset:61696\n"
      "ds_read_b32 v28, %[fa]\n"
      "ds_read_b128 v[96:99], %[rd] offset:0\n"
      "s_waitcnt lgkmcnt(10)\n"
      "v_mfma_f32_32x32x16_bf16 v[112:127], v[164:167], v[52:55], v[112:127]\n"
      "ds_read_b64_tr_b16 v[44:45], %[sa] offset:62464\n"
      "ds_read_b64_tr_b16 v[46:47], %[sa] offset:62720\n"
      "ds_read_b128 v[100:103], %[rd] offset:4096\n"
      "s_waitcnt lgkmcnt(10)\n"
      "v_mfma_f32_32x32x16_bf16 v[112:127], v[168:171], v[32:35], v[112:127]\n"
      "ds_read_b64_tr_b16 v[48:49], %[sa] offset:63488\n"
      "ds_read_b64_tr_b16 v[50:51], %[sa] offset:63744\n"
      "s_waitcnt lgkmcnt(10)\n"
      "v_mfma_f32_32x32x16_bf16 v[112:127], v[172:175], v[36:39], v[112:127]\n"
      "ds_read_b64_tr_b16 v[52:53], %[sa] offset:64512\n"
      "ds_read_b64_tr_b16 v[54:55], %[sa] offset:64768\n"
      "v_cmp_gt_u32 vcc, 22, v28\n"
      "s_waitcnt lgkmcnt(9)\n"
      "v_mfma_f32_32x32x16_bf16 v[112:127], v[176:179], v[40:43], v[112:127]\n"
      "s_cbranch_vccz .LrS3b_%=\n"
      ".LsS3b_%=:\n"
      "ds_read_b32 v28, %[fa]\n"
      "s_addk_i32 %[fuel], -1\n"
      "s_cmp_lt_i32 %[fuel], 0\n"
      "s_waitcnt lgkmcnt(0)\n"
      "s_cbranch_scc1 .LrS3b_%=\n"
      "v_cmp_gt_u32 vcc, 22, v28\n"
      "s_cbranch_vccnz .LsS3b_%=\n"
      ".LrS3b_%=:\n"
      "ds_read_b64_tr_b16 v[32:33], v25 offset:49152\n"
      "ds_read_b64_tr_b16 v[34:35], v25 offset:49408\n"
      "s_waitcnt lgkmcnt(7)\n"
      "v_mfma_f32_32x32x16_bf16 v[112:127], v[180:183], v[44:47], v[112:127]\n"
      "ds_read_b64_tr_b16 v[36:37], v25 offset:50176\n"
      "ds_read_b64_tr_b16 v[38:39], v25 offset:50432\n"
      "s_waitcnt lgkmcnt(6)\n"
      "v_mfma_f32_32x32x16_bf16 v[112:127], v[184:187], v[48:51], v[112:127]\n"
      "ds_read_b64_tr_b16 v[40:41], v25 offset:51200\n"
      "ds_read_b64_tr_b16 v[42:43], v25 offset:51456\n"
      "global_store_dwordx4 %[oo], v[96:99], s[92:93] sc1\n"
      "s_add_u32 s92, s92, 16384\n"
      "s_addc_u32 s93, s93, 0\n"
      "s_waitcnt lgkmcnt(6)\n"
      "v_mfma_f32_32x32x16_bf16 v[112:127], v[188:191], v[52:55], v[112:127]\n"
      "ds_read_b64_tr_b16 v[44:45], v25 offset:52224\n"
      "ds_read_b64_tr_b16 v[46:47], v25 offset:52480\n"
      "global_store_dwordx4 %[oo], v[100:103], s[92:93] sc1\n"
      "s_add_u32 s92, s92, 49152\n"
      "s_addc_u32 s93, s93, 0\n"
      "s_waitcnt lgkmcnt(6)\n"
      "v_mfma_f32_32x32x16_bf16 v[112:127], v[192:195], v[32:35], v[112:127]\n"
      "ds_read_b64_tr_b16 v[48:49], v25 offset:53248\n"
      "ds_read_b64_tr_b16 v[50:51], v25 offset:53504\n"
      "s_waitcnt lgkmcnt(6)\n"
      "v_mfma_f32_32x32x16_bf16 v[112:127], v[196:199], v[36:39], v[112:127]\n"
      "ds_read_b64_tr_b16 v[52:53], v25 offset:54272\n"
      "ds_read_b64_tr_b16 v[54:55], v25 offset:54528\n"
      "s_waitcnt lgkmcnt(6)\n"
      "v_mfma_f32_32x32x16_bf16 v[112:127], v[200:203], v[40:43], v[112:127]\n"
      "ds_read_b64_tr_b16 v[32:33], v25 offset:55296\n"
      "ds_read_b64_tr_b16 v[34:35], v25 offset:55552\n"
      "s_waitcnt lgkmcnt(6)\n"
      "v_mfma_f32_32x32x16_bf16 v[112:127], v[204:207], v[44:47], v[112:127]\n"
      "ds_read_b64_tr_b16 v[36:37], v25 offset:56320\n"
      "ds_read_b64_tr_b16 v[38:39], v25 offset:56576\n"
      "s_waitcnt lgkmcnt(6)\n"
      "v_mfma_f32_32x32x16_bf16 v[112:127], v[208:211], v[48:51], v[112:127]\n"
      "ds_read_b64_tr_b16 v[40:41], v25 offset:57344\n"
      "ds_read_b64_tr_b16 v[42:43], v25 offset:57600\n"
      "s_waitcnt lgkmcnt(6)\n"
      "v_mfma_f32_32x32x16_bf16 v[112:127], v[212:215], v[52:55], v[112:127]\n"
      "ds_read_b64_tr_b16 v[44:45], v25 offset:58368\n"
      "ds_read_b64_tr_b16 v[46:47], v25 offset:58624\n"
      "s_waitcnt lgkmcnt(6)\n"
      "v_mfma_f32_32x32x16_bf16 v[112:127], v[216:219], v[32:35], v[112:127]\n"
      "ds_read_b64_tr_b16 v[48:49], v25 offset:59392\n"
      "ds_read_b64_tr_b16 v[50:51], v25 offset:59648\n"
      "s_waitcnt lgkmcnt(6)\n"
      "v_mfma_f32_32x32x16_bf16 v[112:127], v[220:223], v[36:39], v[112:127]\n"
      "ds_read_b64_tr_b16 v[52:53], v25 offset:60416\n"
      "ds_read_b64_tr_b16 v[54:55], v25 offset:60672\n"
      "s_waitcnt lgkmcnt(6)\n"
      "v_mfma_f32_32x32x16_bf16 v[112:127], v[224:227], v[40:43], v[112:127]\n"
      "ds_read_b64_tr_b16 v[32:33], v25 offset:61440\n"
      "ds_read_b64_tr_b16 v[34:35], v25 offset:61696\n"
      "s_waitcnt lgkmcnt(6)\n"
      "v_mfma_f32_32x32x16_bf16 v[112:127], v[228:231], v[44:47], v[112:127]\n"
      "ds_read_b64_tr_b16 v[36:37], v25 offset:62464\n"
      "ds_read_b64_tr_b16 v[38:39], v25 offset:62720\n"
      "s_waitcnt lgkmcnt(6)\n"
      "v_mfma_f32_32x32x16_bf16 v[112:127], v[232:235], v[48:51], v[112:127]\n"
      "ds_read_b64_tr_b16 v[40:41], v25 offset:63488\n"
      "ds_read_b64_tr_b16 v[42:43], v25 offset:63744\n"
      "s_waitcnt lgkmcnt(6)\n"
      "v_mfma_f32_32x32x16_bf16 v[112:127], v[236:239], v[52:55], v[112:127]\n"
      "ds_read_b64_tr_b16 v[44:45], v25 offset:64512\n"
      "ds_read_b64_tr_b16 v[46:47], v25 offset:64768\n"
      "s_waitcnt lgkmcnt(6)\n"
      "v_mfma_f32_32x32x16_bf16 v[112:127], v[240:243], v[32:35], v[112:127]\n"
      "s_waitcnt lgkmcnt(4)\n"
      "v_mfma_f32_32x32x16_bf16 v[112:127], v[244:247], v[36:39], v[112:127]\n"
      "s_waitcnt lgkmcnt(2)\n"
      "v_mfma_f32_32x32x16_bf16 v[112:127], v[248:251], v[40:43], v[112:127]\n"
      "s_waitcnt lgkmcnt(0)\n"
      "v_mfma_f32_32x32x16_bf16 v[112:127], v[252:255], v[44:47], v[112:127]\n"
      "s_nop 7\n"
      "s_nop 4\n"
      "v_cvt_pk_bf16_f32 v112, v112, v113\n"
      "v_cvt_pk_bf16_f32 v113, v114, v115\n"
      "v_cvt_pk_bf16_f32 v114, v116, v117\n"
      "v_cvt_pk_bf16_f32 v115, v118, v119\n"
      "ds_write_b128 %[wr], v[112:115]\n"
      "v_cvt_pk_bf16_f32 v116, v120, v121\n"
      "v_cvt_pk_bf16_f32 v117, v122, v123\n"
      "v_cvt_pk_bf16_f32 v118, v124, v125\n"
      "v_cvt_pk_bf16_f32 v119, v126, v127\n"
      "ds_write_b128 v26, v[116:119]\n"
      "ds_read_b128 v[120:123], %[rd] offset:0\n"
      "ds_read_b128 v[124:127], %[rd] offset:4096\n"
      "s_waitcnt lgkmcnt(1)\n"
      "global_store_dwordx4 %[oo], v[120:123], s[92:93] sc1\n"
      "s_add_u32 s92, s92, 16384\n"
      "s_addc_u32 s93, s93, 0\n"
      "s_waitcnt lgkmcnt(0)\n"
      "s_mov_b64 exec, %[mo]\n"
      "global_store_dwordx4 %[oo], v[124:127], s[92:93] sc1\n"
      "s_mov_b64 exec, -1\n"
      "s_branch .Lend_%=\n"
      ".LsT0b_%=:\n"
      "ds_read_b32 v28, %[fa]\n"
      "s_addk_i32 %[fuel], -1\n"
      "s_cmp_lt_i32 %[fuel], 0\n"
      "s_waitcnt lgkmcnt(0)\n"
      "s_cbranch_scc1 .LrT0b_%=\n"
      "v_cmp_gt_u32 vcc, 1, v28\n"
      "s_cbranch_vccnz .LsT0b_%=\n"
      "s_branch .LrT0b_%=\n"
      ".LsT1a_%=:\n"
      "ds_read_b32 v28, %[fa]\n"
      "s_addk_i32 %[fuel], -1\n"
      "s_cmp_lt_i32 %[fuel], 0\n"
      "s_waitcnt lgkmcnt(0)\n"
      "s_cbranch_scc1 .LrT1a_%=\n"
      "v_cmp_gt_u32 vcc, 2, v28\n"
      "s_cbranch_vccnz .LsT1a_%=\n"
      "s_branch .LrT1a_%=\n"
      ".LsT1b_%=:\n"
      "ds_read_b32 v28, %[fa]\n"
      "s_addk_i32 %[fuel], -1\n"
      "s_cmp_lt_i32 %[fuel], 0\n"
      "s_waitcnt lgkmcnt(0)\n"
      "s_cbranch_scc1 .LrT1b_%=\n"
      "v_cmp_gt_u32 vcc, 3, v28\n"
      "s_cbranch_vccnz .LsT1b_%=\n"
      "s_branch .LrT1b_%=\n"
      ".LsT2a_%=:\n"
      "ds_read_b32 v28, %[fa]\n"
      "s_addk_i32 %[fuel], -1\n"
      "s_cmp_lt_i32 %[fuel], 0\n"
      "s_waitcnt lgkmcnt(0)\n"
      "s_cbranch_scc1 .LrT2a_%=\n"
      "v_cmp_gt_u32 vcc, 4, v28\n"
      "s_cbranch_vccnz .LsT2a_%=\n"
      "s_branch .LrT2a_%=\n"
      ".LsT2b_%=:\n"
      "ds_read_b32 v28, %[fa]\n"
      "s_addk_i32 %[fuel], -1\n"
      "s_cmp_lt_i32 %[fuel], 0\n"
      "s_waitcnt lgkmcnt(0)\n"
      "s_cbranch_scc1 .LrT2b_%=\n"
      "v_cmp_gt_u32 vcc, 5, v28\n"
      "s_cbranch_vccnz .LsT2b_%=\n"
      "s_branch .LrT2b_%=\n"
      ".LsT3a_%=:\n"
      "ds_read_b32 v28, %[fa]\n"
      "s_addk_i32 %[fuel], -1\n"
      "s_cmp_lt_i32 %[fuel], 0\n"
      "s_waitcnt lgkmcnt(0)\n"
      "s_cbranch_scc1 .LrT3a_%=\n"
      "v_cmp_gt_u32 vcc, 6, v28\n"
      "s_cbranch_vccnz .LsT3a_%=\n"
      "s_branch .LrT3a_%=\n"
      ".LsT3b_%=:\n"
      "ds_read_b32 v28, %[fa]\n"
      "s_addk_i32 %[fuel], -1\n"
      "s_cmp_lt_i32 %[fuel], 0\n"
      "s_waitcnt lgkmcnt(0)\n"
      "s_cbranch_scc1 .LrT3b_%=\n"
      "v_cmp_gt_u32 vcc, 7, v28\n"
      "s_cbranch_vccnz .LsT3b_%=\n"
      "s_branch .LrT3b_%=\n"
      ".LsT4a_%=:\n"
      "ds_read_b32 v28, %[fa]\n"
      "s_addk_i32 %[fuel], -1\n"
      "s_cmp_lt_i32 %[fuel], 0\n"
      "s_waitcnt lgkmcnt(0)\n"
      "s_cbranch_scc1 .LrT4a_%=\n"
      "v_cmp_gt_u32 vcc, 7, v28\n"
      "s_cbranch_vccnz .LsT4a_%=\n"
      "s_branch .LrT4a_%=\n"
      ".LsV0_%=:\n"
      "ds_read_b32 v28, %[fa]\n"
      "s_addk_i32 %[fuel], -1\n"
      "s_cmp_lt_i32 %[fuel], 0\n"
      "s_waitcnt lgkmcnt(0)\n"
      "s_cbranch_scc1 .LrV0_%=\n"
      "v_cmp_gt_u32 vcc, 9, v28\n"
      "s_cbranch_vccnz .LsV0_%=\n"
      "s_branch .LrV0_%=\n"
      ".LsV1_%=:\n"
      "ds_read_b32 v28, %[fa]\n"
      "s_addk_i32 %[fuel], -1\n"
      "s_cmp_lt_i32 %[fuel], 0\n"
      "s_waitcnt lgkmcnt(0)\n"
      "s_cbranch_scc1 .LrV1_%=\n"
      "v_cmp_gt_u32 vcc, 10, v28\n"
      "s_cbranch_vccnz .LsV1_%=\n"
      "s_branch .LrV1_%=\n"
      ".LsV2_%=:\n"
      "ds_read_b32 v28, %[fa]\n"
      "s_addk_i32 %[fuel], -1\n"
      "s_cmp_lt_i32 %[fuel], 0\n"
      "s_waitcnt lgkmcnt(0)\n"
      "s_cbranch_scc1 .LrV2_%=\n"
      "v_cmp_gt_u32 vcc, 11, v28\n"
      "s_cbranch_vccnz .LsV2_%=\n"
      "s_branch .LrV2_%=\n"
      ".LsT4b_%=:\n"
      "ds_read_b32 v28, %[fa]\n"
      "s_addk_i32 %[fuel], -1\n"
      "s_cmp_lt_i32 %[fuel], 0\n"
      "s_waitcnt lgkmcnt(0)\n"
      "s_cbranch_scc1 .LrT4b_%=\n"
      "v_cmp_gt_u32 vcc, 8, v28\n"
      "s_cbranch_vccnz .LsT4b_%=\n"
      "s_branch .LrT4b_%=\n"
      ".LsV3_%=:\n"
      "ds_read_b32 v28, %[fa]\n"
      "s_addk_i32 %[fuel], -1\n"
      "s_cmp_lt_i32 %[fuel], 0\n"
      "s_waitcnt lgkmcnt(0)\n"
      "s_cbranch_scc1 .LrV3_%=\n"
      "v_cmp_gt_u32 vcc, 12, v28\n"
      "s_cbranch_vccnz .LsV3_%=\n"
      "s_branch .LrV3_%=\n"
      ".LsV4_%=:\n"
      "ds_read_b32 v28, %[fa]\n"
      "s_addk_i32 %[fuel], -1\n"
      "s_cmp_lt_i32 %[fuel], 0\n"
      "s_waitcnt lgkmcnt(0)\n"
      "s_cbranch_scc1 .LrV4_%=\n"
      "v_cmp_gt_u32 vcc, 12, v28\n"
      "s_cbranch_vccnz .LsV4_%=\n"
      "s_branch .LrV4_%=\n"
      ".LsV5_%=:\n"
      "ds_read_b32 v28, %[fa]\n"
      "s_addk_i32 %[fuel], -1\n"
      "s_cmp_lt_i32 %[fuel], 0\n"
      "s_waitcnt lgkmcnt(0)\n"
      "s_cbranch_scc1 .LrV5_%=\n"
      "v_cmp_gt_u32 vcc, 14, v28\n"
      "s_cbranch_vccnz .LsV5_%=\n"
      "s_branch .LrV5_%=\n"
      ".LsV6_%=:\n"
      "ds_read_b32 v28, %[fa]\n"
      "s_addk_i32 %[fuel], -1\n"
      "s_cmp_lt_i32 %[fuel], 0\n"
      "s_waitcnt lgkmcnt(0)\n"
      "s_cbranch_scc1 .LrV6_%=\n"
      "v_cmp_gt_u32 vcc, 15, v28\n"
      "s_cbranch_vccnz .LsV6_%=\n"
      "s_branch .LrV6_%=\n"
      ".LsV7_%=:\n"
      "ds_read_b32 v28, %[fa]\n"
      "s_addk_i32 %[fuel], -1\n"
      "s_cmp_lt_i32 %[fuel], 0\n"
      "s_waitcnt lgkmcnt(0)\n"
      "s_cbranch_scc1 .LrV7_%=\n"
      "v_cmp_gt_u32 vcc, 16, v28\n"
      "s_cbranch_vccnz .LsV7_%=\n"
      "s_branch .LrV7_%=\n"
      ".Lend_%=:\n"
      : [fuel] "+s"(fuel)
      : [xa] "v"(xa), [wa] "v"(wa), [ha] "v"(ha), [hh] "v"(hq), [fa] "v"(fa), [sg] "v"(sg), [wv] "v"(wv), [sa] "v"(sa),
        [wr] "v"(wr), [rd] "v"(rd), [oo] "v"(oo), [ob] "s"(out + (rb * 250 + (cs & 1) * 32) * 1024), [mf] "s"(mf),
        [ml] "s"(ml), [mo] "s"(mo)
      : "vcc", "scc", "s90", "s92", "s93", "s98", "s99", "memory", "v16", "v17", "v18", "v19", "v20", "v21", "v22", "v23", "v24", "v25", "v26", "v27", "v28", "v29", "v30", "v31", "v32", "v33", "v34", "v35", "v36", "v37", "v38", "v39", "v40", "v41", "v42", "v43", "v44", "v45", "v46", "v47", "v48", "v49", "v50", "v51", "v52", "v53", "v54", "v55", "v56", "v57", "v58", "v59", "v60", "v61", "v62", "v63", "v64", "v65", "v66", "v67", "v68", "v69", "v70", "v71", "v72", "v73", "v74", "v75", "v76", "v77", "v78", "v79", "v80", "v81", "v82", "v83", "v84", "v85", "v86", "v87", "v88", "v89", "v90", "v91", "v92", "v93", "v94", "v95", "v96", "v97", "v98", "v99", "v100", "v101", "v102", "v103", "v104", "v105", "v106", "v107", "v108", "v109", "v110", "v111", "v112", "v113", "v114", "v115", "v116", "v117", "v118", "v119", "v120", "v121", "v122", "v123", "v124", "v125", "v126", "v127", "v128", "v129", "v130", "v131", "v132", "v133", "v134", "v135", "v136", "v137", "v138", "v139", "v140", "v141", "v142", "v143", "v144", "v145", "v146", "v147", "v148", "v149", "v150", "v151", "v152", "v153", "v154", "v155", "v156", "v157", "v158", "v159", "v160", "v161", "v162", "v163", "v164", "v165", "v166", "v167", "v168", "v169", "v170", "v171", "v172", "v173", "v174", "v175", "v176", "v177", "v178", "v179", "v180", "v181", "v182", "v183", "v184", "v185", "v186", "v187", "v188", "v189", "v190", "v191", "v192", "v193", "v194", "v195", "v196", "v197", "v198", "v199", "v200", "v201", "v202", "v203", "v204", "v205", "v206", "v207", "v208", "v209", "v210", "v211", "v212", "v213", "v214", "v215", "v216", "v217", "v218", "v219", "v220", "v221", "v222", "v223", "v224", "v225", "v226", "v227", "v228", "v229", "v230", "v231", "v232", "v233", "v234", "v235", "v236", "v237", "v238", "v239", "v240", "v241", "v242", "v243", "v244", "v245", "v246", "v247", "v248", "v249", "v250", "v251", "v252", "v253", "v254", "v255");
    __builtin_amdgcn_endpgm();
  }
}

extern "C" void kernel_launch(void* const* d_in, const int* in_sizes, int n_in, void* d_out, int out_size,
                              void* d_ws, size_t ws_size, hipStream_t stream) {
  (void)hipFuncSetAttribute((const void*)fused, hipFuncAttributeMaxDynamicSharedMemorySize, L_SIZE);
  fused<<<256, 512, L_SIZE, stream>>>((const char*)d_in[0], (const char*)d_in[1], (const char*)d_in[2],
                                      (const char*)d_in[3], (char*)d_out, (char*)d_ws);
}
```
